```python
import torch

def _softmax(x, axis=-1):
    return torch.softmax(x, dim=axis)

def _gelu(x):
    return torch.nn.functional.gelu(x, approximate='tanh')

def _silu(x):
    return torch.nn.functional.silu(x)

def _sigmoid(x):
    return torch.sigmoid(x)

def _layernorm(x, axis=-1, eps=1e-05):
    m = x.mean(dim=axis, keepdim=True)
    v = x.var(dim=axis, keepdim=True, unbiased=False)
    return (x - m) / torch.sqrt(v + eps)

def _scatter_add(tgt, idx, src, axis=0):
    return tgt.clone().index_add_(axis, idx, src)

def _segment_sum(src, seg, n):
    out = torch.zeros((n,) + src.shape[1:], dtype=src.dtype)
    return out.index_add_(0, seg, src)

def _pad(x, pw):
    flat = tuple((v for pair in reversed(pw) for v in pair))
    return torch.nn.functional.pad(x, flat)
kernel_name = 'adaLN_cond__sliding_block_cached__geglu_ffn__decode'

def setup_inputs():

    _w = lambda a: (lambda t: t.to(torch.bfloat16) if t.is_floating_point() else t)(torch.as_tensor(a))

    def _A_setup():
        B, Sq, D = (1, 1, 512)
        Hq, Hkv, Sk, Dh = (32, 8, 65536, 128)
        assert Hq % Hkv == 0, (Hq, Hkv)
        return {'x': _w((lambda *s: torch.randn(*s, dtype=torch.bfloat16) * 0.1)(B, Sq, D)), 'k_cache': _w((lambda *s: torch.randn(*s, dtype=torch.bfloat16) * 0.8)(B, Hkv, Sk, Dh)), 'v_cache': _w((lambda *s: torch.randn(*s, dtype=torch.bfloat16) * 1.6)(B, Hkv, Sk, Dh)), 'wq': _w((lambda *s: torch.randn(*s, dtype=torch.bfloat16) * 0.8)(D, Hq * Dh) * D ** (-0.5)), 'wo': _w((lambda *s: torch.randn(*s, dtype=torch.bfloat16) * 1.6)(Hq * Dh, D) * (Hq * Dh) ** (-0.5))}
    _A_in = _A_setup()

    def _B_setup():
        B, S, D, F = (1, 1, 512, 1792)
        return {'x': _w((lambda *s: torch.randn(*s, dtype=torch.bfloat16) * 0.1)(B, S, D)), 'w_gate': _w((lambda *s: torch.randn(*s, dtype=torch.bfloat16) * 0.2)(D, F)), 'w_up': _w((lambda *s: torch.randn(*s, dtype=torch.bfloat16) * 0.2)(D, F)), 'w_down': _w((lambda *s: torch.randn(*s, dtype=torch.bfloat16) * 0.2)(F, D))}
    _B_in = _B_setup()
    return {'A_x': _A_in['x'], 'A_k_cache': _A_in['k_cache'], 'A_v_cache': _A_in['v_cache'], 'A_wq': _A_in['wq'], 'A_wo': _A_in['wo'], 'B_x': _B_in['x'], 'B_w_gate': _B_in['w_gate'], 'B_w_up': _B_in['w_up'], 'B_w_down': _B_in['w_down'], 'c': _w((lambda *s: torch.randn(*s, dtype=torch.bfloat16) * 0.4)(1, 512) * 8.0), 'w_mod': _w((lambda *s: torch.randn(*s, dtype=torch.bfloat16) * 0.4)(512, 6 * 512) * 512 ** (-0.5) * 8.0)}

def reference(A_x, A_k_cache, A_v_cache, A_wq, A_wo, B_x, B_w_gate, B_w_up, B_w_down, c, w_mod):

    def _A_ref(x, k_cache, v_cache, wq, wo):
        B, Sq, D = x.shape
        Hq, Dh = (32, 128)
        Hkv, Sk = (k_cache.shape[1], k_cache.shape[2])
        W = min(4096, max(256, Sk // 4))
        rep = Hq // Hkv
        q = (lambda x, *d: x.permute(*d))((x @ wq).reshape(B, Sq, Hq, Dh), 0, 2, 1, 3)
        k = (lambda x, n, axis: torch.repeat_interleave(x, n, dim=axis))(k_cache[:, :, Sk - W:, :], rep, axis=1)
        v = (lambda x, n, axis: torch.repeat_interleave(x, n, dim=axis))(v_cache[:, :, Sk - W:, :], rep, axis=1)
        scores = torch.einsum('bhqd,bhkd->bhqk', q, k) * Dh ** (-0.5)
        p = _softmax(scores, axis=-1)
        o = torch.einsum('bhqk,bhkd->bhqd', p, v)
        return (lambda x, *d: x.permute(*d))(o, 0, 2, 1, 3).reshape(B, Sq, Hq * Dh) @ wo

    def _B_ref(x, w_gate, w_up, w_down):
        return _gelu(x @ w_gate) * (x @ w_up) @ w_down
    x = A_x
    mod = (_silu(c) @ w_mod).reshape(c.shape[0], 6, 1, -1)
    sa, ca, ga, sb, cb, gb = (mod[:, 0].clone(), mod[:, 1].clone(), mod[:, 2].clone(), mod[:, 3].clone(), mod[:, 4].clone(), mod[:, 5].clone())
    h = _layernorm(x, axis=-1) * (1 + ca) + sa
    x = x + ga * _A_ref(h, A_k_cache, A_v_cache, A_wq, A_wo)
    h = _layernorm(x, axis=-1) * (1 + cb) + sb
    x = x + gb * _B_ref(h, B_w_gate, B_w_up, B_w_down)
    return x
```

```cpp
#include <hip/hip_runtime.h>
#include <hip/hip_bf16.h>
#include <stdint.h>

typedef unsigned short u16;
static __device__ __forceinline__ float bf2f(u16 b) { return __builtin_bit_cast(float, (unsigned)b << 16); }
static __device__ __forceinline__ float rbf(float f) {
  __bf16 b = (__bf16)f; return (float)b; }
static __device__ __forceinline__ u16 f2bf(float f) { __bf16 b = (__bf16)f; return __builtin_bit_cast(u16, b); }

constexpr int D = 512, HQ = 32, HKV = 8, SK = 65536, DH = 128, W = 4096, F = 1792;

__global__ void k_mod(const u16* c, const u16* wmod, float* mod) {
  int n = blockIdx.x * 64 + threadIdx.x;
  float acc = 0.f;
  for (int j = 0; j < D; ++j) {
    float cv = bf2f(c[j]);
    float s = rbf(cv / (1.f + __expf(-cv)));
    acc += s * bf2f(wmod[(size_t)j * 3072 + n]);
  }
  mod[n] = rbf(acc);
}

__device__ float ln_mod(float xv, float cm, float sm, float* red) {
  int t = threadIdx.x;
  red[t] = xv; __syncthreads();
  float s = 0.f; for (int i = 0; i < D; ++i) s += red[i];
  float mean = s / D;
  float v = 0.f; for (int i = 0; i < D; ++i) { float d = red[i] - mean; v += d * d; }
  v = rbf(v / D);
  float m = rbf(mean);
  float den = rbf(sqrtf(rbf(v + 1e-5f)));
  float ln = rbf(rbf(xv - m) / den);
  __syncthreads();
  return rbf(rbf(ln * rbf(1.f + cm)) + sm);
}

__global__ void k_h1(const u16* x, const float* mod, float* h) {
  __shared__ float red[D];
  int t = threadIdx.x;
  h[t] = ln_mod(bf2f(x[t]), mod[512 + t], mod[t], red);
}

__global__ void k_gemv(const float* in, const u16* w, float* out, int K, int N) {
  int n = blockIdx.x * 64 + threadIdx.x;
  float acc = 0.f;
  for (int k = 0; k < K; ++k) acc += in[k] * bf2f(w[(size_t)k * N + n]);
  out[n] = rbf(acc);
}

__global__ void k_attn(const float* q, const u16* kc, const u16* vc, float* o) {
  __shared__ float sc[W];
  __shared__ float red[256];
  int h = blockIdx.x, g = h / 4, t = threadIdx.x;
  const u16* kb = kc + ((size_t)g * SK + (SK - W)) * DH;
  const u16* vb = vc + ((size_t)g * SK + (SK - W)) * DH;
  float mx = -INFINITY;
  for (int j = t; j < W; j += 256) {
    float acc = 0.f;
    for (int d = 0; d < DH; ++d) acc += q[h * DH + d] * bf2f(kb[(size_t)j * DH + d]);
    float s = rbf(rbf(acc) * 0.08838834764831845f);
    sc[j] = s; mx = fmaxf(mx, s);
  }
  red[t] = mx; __syncthreads();
  for (int i = 0; i < 256; ++i) mx = fmaxf(mx, red[i]);
  __syncthreads();
  float sum = 0.f;
  for (int j = t; j < W; j += 256) { float e = __expf(sc[j] - mx); sc[j] = e; sum += e; }
  red[t] = sum; __syncthreads();
  sum = 0.f; for (int i = 0; i < 256; ++i) sum += red[i];
  float inv = 1.f / sum;
  if (t < DH) {
    float acc = 0.f;
    for (int j = 0; j < W; ++j) acc += rbf(sc[j] * inv) * bf2f(vb[(size_t)j * DH + t]);
    o[h * DH + t] = rbf(acc);
  }
}

__global__ void k_h2(const u16* x, const float* mod, const float* a, float* x1, float* h) {
  __shared__ float red[D];
  int t = threadIdx.x;
  float xv = rbf(bf2f(x[t]) + rbf(mod[1024 + t] * a[t]));
  x1[t] = xv;
  h[t] = ln_mod(xv, mod[2048 + t], mod[1536 + t], red);
}

__global__ void k_act(const float* g, const float* u, float* act) {
  int n = blockIdx.x * 64 + threadIdx.x;
  float gv = g[n];
  float inner = 0.7978845608028654f * (gv + 0.044715f * gv * gv * gv);
  float ge = rbf(0.5f * gv * (1.f + tanhf(inner)));
  act[n] = rbf(ge * u[n]);
}

__global__ void k_out(const float* x1, const float* mod, const float* f, u16* out) {
  int t = threadIdx.x;
  out[t] = f2bf(x1[t] + rbf(mod[2560 + t] * f[t]));
}

extern "C" void kernel_launch(void* const* d_in, const int* in_sizes, int n_in,
                              void* d_out, int out_size, void* d_ws, size_t ws_size,
                              hipStream_t stream) {
  const u16* x = (const u16*)d_in[0];
  const u16* kc = (const u16*)d_in[1];
  const u16* vc = (const u16*)d_in[2];
  const u16* wq = (const u16*)d_in[3];
  const u16* wo = (const u16*)d_in[4];
  const u16* wg = (const u16*)d_in[6];
  const u16* wu = (const u16*)d_in[7];
  const u16* wd = (const u16*)d_in[8];
  const u16* c = (const u16*)d_in[9];
  const u16* wmod = (const u16*)d_in[10];
  float* ws = (float*)d_ws;
  float *mod = ws, *h = ws + 3072, *q = h + 512, *o = q + 4096, *a = o + 4096, *x1 = a + 512, *h2 = x1 + 512,
        *g = h2 + 512, *u = g + F, *act = u + F, *f = act + F;
  k_mod<<<48, 64, 0, stream>>>(c, wmod, mod);
  k_h1<<<1, 512, 0, stream>>>(x, mod, h);
  k_gemv<<<64, 64, 0, stream>>>(h, wq, q, 512, 4096);
  k_attn<<<32, 256, 0, stream>>>(q, kc, vc, o);
  k_gemv<<<8, 64, 0, stream>>>(o, wo, a, 4096, 512);
  k_h2<<<1, 512, 0, stream>>>(x, mod, a, x1, h2);
  k_gemv<<<28, 64, 0, stream>>>(h2, wg, g, 512, F);
  k_gemv<<<28, 64, 0, stream>>>(h2, wu, u, 512, F);
  k_act<<<28, 64, 0, stream>>>(g, u, act);
  k_gemv<<<8, 64, 0, stream>>>(act, wd, f, F, 512);
  k_out<<<1, 512, 0, stream>>>(x1, mod, f, (u16*)d_out);
}
```

```cpp
#include <hip/hip_runtime.h>
#include <stdint.h>

#define GAS __attribute__((address_space(1)))
typedef unsigned short u16;
typedef unsigned u32;
typedef unsigned long long u64;
typedef u32 v4u __attribute__((ext_vector_type(4)));
typedef u32 v2u __attribute__((ext_vector_type(2)));
typedef float v4f __attribute__((ext_vector_type(4)));
typedef float v2f __attribute__((ext_vector_type(2)));
typedef __bf16 bf2 __attribute__((ext_vector_type(2)));
typedef __bf16 bf8 __attribute__((ext_vector_type(8)));
typedef __amdgpu_buffer_rsrc_t rsrc_t;

#define NT_KV 1
#define NT_W 0
#define WO_SLEEP 0

constexpr int D = 512, SK = 65536, DH = 128, WIN = 4096, FF = 1792, NMOD = 3072;
constexpr int OFF_EPOCH = 0;
constexpr int OFF_S1 = 4096;
constexpr int OFF_S2 = 8192;
constexpr int OFF_ML = 24576;
constexpr int OFF_MODR = 28672;
constexpr int OFF_S4 = 32768;
constexpr int OFF_S3 = 65536;
constexpr int OFF_S6 = OFF_S3 + 8 * 16 * 4 * 128 * 4;
constexpr int WS_BYTES = OFF_S6 + 112 * 512 * 4;
constexpr int OOB = 0x7FFFFFF0;
constexpr u32 SENT = 0xFFFFFFFFu;
constexpr int AUX_SC1 = 16;
constexpr u32 SPIN_CAP = 1u << 17;
constexpr int LDS_BYTES = 84 * 1024;

#define STAMP(i) do {} while (0)

static __device__ __forceinline__ float lo2f(u32 d) { return __builtin_bit_cast(float, d << 16); }
static __device__ __forceinline__ float hi2f(u32 d) { return __builtin_bit_cast(float, d & 0xffff0000u); }
static __device__ __forceinline__ float rbf(float f) { return (float)(__bf16)f; }
static __device__ __forceinline__ u32 pk2(float lo, float hi) {
  v2f v = {lo, hi}; return __builtin_bit_cast(u32, __builtin_convertvector(v, bf2)); }
static __device__ __forceinline__ float dot2(u32 a, u32 b, float c) {
  return __builtin_amdgcn_fdot2_f32_bf16(__builtin_bit_cast(bf2, a), __builtin_bit_cast(bf2, b), c, false); }
static __device__ __forceinline__ v4f mfma(v4u a, v4u b, v4f c) {
  return __builtin_amdgcn_mfma_f32_16x16x32_bf16(__builtin_bit_cast(bf8, a), __builtin_bit_cast(bf8, b), c, 0, 0, 0); }
static __device__ __forceinline__ u32 f2u(float f) { return __builtin_bit_cast(u32, f); }
static __device__ __forceinline__ float u2f(u32 u) { return __builtin_bit_cast(float, u); }
static __device__ __forceinline__ u32 canon(u32 u) { return u == SENT ? 0x7FC00000u : u; }
template <int CTRL> static __device__ __forceinline__ float dpp(float x) {
  return u2f(__builtin_amdgcn_mov_dpp(f2u(x), CTRL, 0xf, 0xf, true)); }
constexpr int XOR1 = 0xB1, XOR2 = 0x4E, HMIR = 0x141, ROR8 = 0x128, ROR4 = 0x124;
static __device__ __forceinline__ float tr32(float a, float b) {
  auto r = __builtin_amdgcn_permlane32_swap(f2u(a), f2u(b), false, false); return u2f(r[0]) + u2f(r[1]); }
static __device__ __forceinline__ float tr16(float a, float b) {
  auto r = __builtin_amdgcn_permlane16_swap(f2u(a), f2u(b), false, false); return u2f(r[0]) + u2f(r[1]); }
static __device__ __forceinline__ float xsum_rows(float x) { x = tr16(x, x); return tr32(x, x); }
static __device__ __forceinline__ float xmax_rows(float x) {
  auto r = __builtin_amdgcn_permlane16_swap(f2u(x), f2u(x), false, false); x = fmaxf(u2f(r[0]), u2f(r[1]));
  auto s = __builtin_amdgcn_permlane32_swap(f2u(x), f2u(x), false, false); return fmaxf(u2f(s[0]), u2f(s[1])); }
static __device__ __forceinline__ float wave_sum(float x) {
  x += dpp<XOR1>(x); x += dpp<XOR2>(x); x += dpp<HMIR>(x); x += dpp<ROR8>(x); return xsum_rows(x); }

template <int NT> static __device__ __forceinline__ v4u ldwt(const u16* p) {
  const GAS v4u* g = (const GAS v4u*)p;
  if constexpr (NT) return __builtin_nontemporal_load(g); else return *g; }
#define ldw(p, nt) ldwt<nt>(p)
static __device__ __forceinline__ v4u ld128(rsrc_t rs, int off) { return __builtin_amdgcn_raw_buffer_load_b128(rs, off, 0, AUX_SC1); }
static __device__ __forceinline__ u32 ld32(rsrc_t rs, int off) { return __builtin_amdgcn_raw_buffer_load_b32(rs, off, 0, AUX_SC1); }
static __device__ __forceinline__ void st128(rsrc_t rs, int off, v4u v) { __builtin_amdgcn_raw_buffer_store_b128(v, rs, off, 0, AUX_SC1); }
static __device__ __forceinline__ void st64(rsrc_t rs, int off, v2u v) { __builtin_amdgcn_raw_buffer_store_b64(v, rs, off, 0, AUX_SC1); }
static __device__ __forceinline__ void st32(rsrc_t rs, int off, u32 v) { __builtin_amdgcn_raw_buffer_store_b32(v, rs, off, 0, AUX_SC1); }
static __device__ __forceinline__ u32 umax3(u32 a, u32 b, u32 c) { return max(max(a, b), c); }
static __device__ __forceinline__ u32 mx4(v4u v) { return max(umax3(v.x, v.y, v.z), v.w); }
#define MEMBAR() asm volatile("" ::: "memory")
#define PIN4(v) asm volatile("" : "+v"(v))

struct Tile48 { v4u lo01, hi01, lo23, hi23; };
static __device__ __forceinline__ v4u plo(v4u a, v4u b) {
  return v4u{__builtin_amdgcn_perm(b.x, a.x, 0x05040100), __builtin_amdgcn_perm(b.y, a.y, 0x05040100),
             __builtin_amdgcn_perm(b.z, a.z, 0x05040100), __builtin_amdgcn_perm(b.w, a.w, 0x05040100)}; }
static __device__ __forceinline__ v4u phi(v4u a, v4u b) {
  return v4u{__builtin_amdgcn_perm(b.x, a.x, 0x07060302), __builtin_amdgcn_perm(b.y, a.y, 0x07060302),
             __builtin_amdgcn_perm(b.z, a.z, 0x07060302), __builtin_amdgcn_perm(b.w, a.w, 0x07060302)}; }
static __device__ __forceinline__ Tile48 pair_rows(const v4u (&w)[4]) {
  Tile48 t; t.lo01 = plo(w[0], w[1]); t.hi01 = phi(w[0], w[1]); t.lo23 = plo(w[2], w[3]); t.hi23 = phi(w[2], w[3]);
  PIN4(t.lo01); PIN4(t.hi01); PIN4(t.lo23); PIN4(t.hi23); return t; }
static __device__ __forceinline__ void gemv_tile(const Tile48& t, u32 a01, u32 a23, float* redw, int lane) {
  float c[8];
#pragma unroll
  for (int i = 0; i < 4; ++i) {
    c[2 * i] = dot2(t.lo23[i], a23, dot2(t.lo01[i], a01, 0.f));
    c[2 * i + 1] = dot2(t.hi23[i], a23, dot2(t.hi01[i], a01, 0.f));
  }
  float d0 = tr32(c[0], c[4]), d1 = tr32(c[1], c[5]), d2 = tr32(c[2], c[6]), d3 = tr32(c[3], c[7]);
  float e0 = tr16(d0, d2), e1 = tr16(d1, d3);
  e0 += dpp<ROR4>(e0); e1 += dpp<ROR4>(e1); e0 += dpp<ROR8>(e0); e1 += dpp<ROR8>(e1);
  if ((lane & 12) == 0) *(v2f*)(redw + 8 * (lane & 3) + 2 * ((lane >> 4) & 1) + 4 * (lane >> 5)) = v2f{e0, e1};
}
static __device__ __forceinline__ float red8(const float (&p)[8], int lane) {
  float d0 = tr32(p[0], p[4]), d1 = tr32(p[1], p[5]), d2 = tr32(p[2], p[6]), d3 = tr32(p[3], p[7]);
  float e0 = tr16(d0, d2), e1 = tr16(d1, d3);
  const bool b3 = (lane & 8) != 0;
  float keep = b3 ? e1 : e0, send = b3 ? e0 : e1;
  float r = keep + dpp<ROR8>(send);
  r += dpp<HMIR>(r); r += dpp<XOR2>(r); r += dpp<XOR1>(r);
  return r;
}
static __device__ __forceinline__ void mul8(float s, v4u w, float (&p)[8]) {
#pragma unroll
  for (int i = 0; i < 4; ++i) { p[2 * i] = s * lo2f(w[i]); p[2 * i + 1] = s * hi2f(w[i]); }
}
static __device__ __forceinline__ int tidx(int lane) { return 4 * (lane & 15) + 2 * (lane >> 5) + ((lane >> 4) & 1); }
static __device__ __forceinline__ float tr4(v4f v) { return tr16(tr32(v.x, v.z), tr32(v.y, v.w)); }
static __device__ __forceinline__ int trow(int lane, int t) { return 16 * ((lane >> 2) & 3) + 4 * t + 2 * (lane >> 5) + ((lane >> 4) & 1); }

__global__ void __launch_bounds__(512, 2)
fused_block(const u16* __restrict__ x, const u16* __restrict__ kc, const u16* __restrict__ vc, const u16* __restrict__ wq,
            const u16* __restrict__ wo, const u16* __restrict__ wg, const u16* __restrict__ wu, const u16* __restrict__ wd,
            const u16* __restrict__ cnd, const u16* __restrict__ wmod, u16* __restrict__ out, unsigned char* ws) {
  __shared__ __attribute__((aligned(16))) unsigned char lds[LDS_BYTES];
  const int tid = threadIdx.x, lane = tid & 63, w = __builtin_amdgcn_readfirstlane(tid >> 6);
  const int bid = blockIdx.x, g = bid & 7, idx = bid >> 3, j = idx >> 1;
  const rsrc_t rs = __builtin_amdgcn_make_buffer_rsrc(ws, 0, WS_BYTES, 0x00020000);
  STAMP(0);
  asm volatile("" :: "s"(x), "s"(kc), "s"(vc), "s"(wq), "s"(wo), "s"(wg), "s"(wu), "s"(wd), "s"(cnd), "s"(wmod), "s"(out));
  u32 tag = ld32(rs, OFF_EPOCH);
  float* red = (float*)lds;
  constexpr float LOG2E = 1.4426950408889634f;

  if ((idx & 1) == 0) {
    const int qi = g * 16 + j;
    {
      int off = OOB;
      if (tid < 128) off = OFF_S3 + qi * 2048 + tid * 16;
      else if (tid < 256) off = qi < 112 ? OFF_S6 + qi * 2048 + (tid - 128) * 16 : OOB;
      else if (tid < 264) off = OFF_S4 + (g * 512 + 32 * j) * 4 + (tid - 256) * 16;
      else if (tid < 266) off = OFF_ML + qi * 32 + (tid - 264) * 16;
      else if (tid < 268) off = OFF_MODR + (256 * g + 16 * j) * 2 + (tid - 266) * 16;
      if (w < 5) st128(rs, off, v4u{SENT, SENT, SENT, SENT});
      MEMBAR();
    }
    const u32 craw = cnd[tid];
    const v4u wm = ldw(wmod + (size_t)tid * NMOD + 128 * g + 8 * j, NT_W);
    const v4u xall = *(const GAS v4u*)(x + 8 * lane);
    const int cc = lane & 3, rb = lane >> 2;
    const v2u xown = *(const GAS v2u*)(x + 64 * w + 4 * rb);
    v4u wt[4];
#pragma unroll
    for (int t = 0; t < 4; ++t) wt[t] = ldw(wq + (size_t)(64 * w + 4 * rb + t) * 4096 + 512 * g + 32 * j + 8 * cc, NT_W);
    STAMP(1);
    asm volatile("" : "+v"(tag)); tag += 1u;
    const float cv = lo2f(craw);
    const float sil = rbf(cv / (1.f + __expf(-cv)));
    { float p[8]; mul8(sil, wm, p);
      const float r = red8(p, lane);
      asm volatile("s_waitcnt vmcnt(6)" ::: "memory");
      if ((lane & 7) == 0) red[w * 8 + (lane >> 3)] = r; }
    STAMP(2);
    __syncthreads();
    if (w == 0) {
      float a = 0.f;
#pragma unroll
      for (int k = 0; k < 8; ++k) a += red[k * 8 + (lane & 7)];
      const float b = dpp<XOR1>(a);
      const u32 pr = pk2(a, b);
      const u32 pr2 = f2u(dpp<XOR2>(u2f(pr)));
      if (lane == 0 || lane == 4) st128(rs, OFF_S1 + (64 * g + 4 * j) * 8 + lane * 4, v4u{pr, tag, pr2, tag});
    }
    STAMP(3);
    float ln[4];
    { float s = 0.f;
#pragma unroll
      for (int i = 0; i < 4; ++i) s += lo2f(xall[i]) + hi2f(xall[i]);
      const float mean = wave_sum(s) * (1.f / D);
      float v = 0.f;
#pragma unroll
      for (int i = 0; i < 4; ++i) { const float a = lo2f(xall[i]) - mean, b = hi2f(xall[i]) - mean; v += a * a + b * b; }
      const float var = rbf(wave_sum(v) * (1.f / D));
      const float m = rbf(mean), den = rbf(sqrtf(rbf(var + 1e-5f)));
      ln[0] = rbf(rbf(lo2f(xown.x) - m) / den); ln[1] = rbf(rbf(hi2f(xown.x) - m) / den);
      ln[2] = rbf(rbf(lo2f(xown.y) - m) / den); ln[3] = rbf(rbf(hi2f(xown.y) - m) / den); }
    Tile48 T = pair_rows(wt);
    STAMP(4);
    v4u sa, ca;
    for (u32 spin = 0; spin < SPIN_CAP; ++spin) {
      MEMBAR();
      sa = ld128(rs, OFF_S1 + (32 * w + 2 * rb) * 8);
      ca = ld128(rs, OFF_S1 + (256 + 32 * w + 2 * rb) * 8);
      if (__all(((sa.y ^ tag) | (sa.w ^ tag) | (ca.y ^ tag) | (ca.w ^ tag)) == 0u)) break;
    }
    STAMP(5);
    { const float h0 = rbf(ln[0] * rbf(1.f + lo2f(ca.x))) + lo2f(sa.x), h1 = rbf(ln[1] * rbf(1.f + hi2f(ca.x))) + hi2f(sa.x);
      const float h2 = rbf(ln[2] * rbf(1.f + lo2f(ca.z))) + lo2f(sa.z), h3 = rbf(ln[3] * rbf(1.f + hi2f(ca.z))) + hi2f(sa.z);
      gemv_tile(T, pk2(h0, h1), pk2(h2, h3), red + 256 + w * 32, lane); }
    __syncthreads();
    if (w == 0) {
      v4f a = {0.f, 0.f, 0.f, 0.f};
#pragma unroll
      for (int k = 0; k < 8; ++k) a += *(const v4f*)(red + 256 + k * 32 + 4 * (lane & 7));
      if (lane < 8) st128(rs, OFF_S2 + (g * 256 + 16 * j + 2 * lane) * 8, v4u{pk2(a.x, a.y), tag, pk2(a.z, a.w), tag});
    }
    STAMP(6);
    __syncthreads();
    const v4u wr0 = ldw(wmod + (size_t)tid * NMOD + 1024 + 256 * g + 16 * j, NT_W);
    const v4u wr1 = ldw(wmod + (size_t)tid * NMOD + 1024 + 256 * g + 16 * j + 8, NT_W);
#pragma unroll
    for (int t = 0; t < 4; ++t) wt[t] = ldw(wo + (size_t)(512 * g + 64 * w + trow(lane, t)) * D + 32 * j + 8 * cc, NT_W);
    { float p[8]; mul8(sil, wr0, p); const float r0 = red8(p, lane); mul8(sil, wr1, p); const float r1 = red8(p, lane);
      if ((lane & 7) == 0) { red[w * 16 + (lane >> 3)] = r0; red[w * 16 + 8 + (lane >> 3)] = r1; } }
    __syncthreads();
    if (w == 0) {
      float a = 0.f;
#pragma unroll
      for (int k = 0; k < 8; ++k) a += red[k * 16 + (lane & 15)];
      const u32 pr = canon(pk2(a, dpp<XOR1>(a)));
      if (lane < 16 && (lane & 1) == 0) st32(rs, OFF_MODR + (256 * g + 16 * j + lane) * 2, pr);
    }
    T = pair_rows(wt);
    STAMP(7);
    float oval;
    { const int cq = lane >> 4, dq = lane & 15, head = w >> 1;
      v4u o4[4], ml0, ml1;
      for (u32 spin = 0; spin < SPIN_CAP; ++spin) {
        MEMBAR();
        ml0 = ld128(rs, OFF_ML + ((g * 4 + head) * 16 + 4 * cq) * 8);
        ml1 = ld128(rs, OFF_ML + ((g * 4 + head) * 16 + 4 * cq) * 8 + 16);
#pragma unroll
        for (int t = 0; t < 4; ++t) o4[t] = ld128(rs, OFF_S3 + (((g * 16 + 4 * cq + t) * 4 + head) * 128 + 64 * (w & 1) + 4 * dq) * 4);
        if (__all(max(umax3(mx4(ml0), mx4(ml1), mx4(o4[0])), umax3(mx4(o4[1]), mx4(o4[2]), mx4(o4[3]))) != SENT)) break;
      }
      STAMP(8);
      const float m0 = u2f(ml0.x), m1 = u2f(ml0.z), m2 = u2f(ml1.x), m3 = u2f(ml1.z);
      const float M = xmax_rows(fmaxf(fmaxf(m0, m1), fmaxf(m2, m3)));
      const float w0 = __builtin_amdgcn_exp2f((m0 - M) * LOG2E), w1 = __builtin_amdgcn_exp2f((m1 - M) * LOG2E);
      const float w2 = __builtin_amdgcn_exp2f((m2 - M) * LOG2E), w3 = __builtin_amdgcn_exp2f((m3 - M) * LOG2E);
      const float L = xsum_rows(w0 * u2f(ml0.y) + w1 * u2f(ml0.w) + w2 * u2f(ml1.y) + w3 * u2f(ml1.w));
      const v4f acc = w0 * __builtin_bit_cast(v4f, o4[0]) + w1 * __builtin_bit_cast(v4f, o4[1]) + w2 * __builtin_bit_cast(v4f, o4[2]) + w3 * __builtin_bit_cast(v4f, o4[3]);
      oval = tr4(acc) * __builtin_amdgcn_rcpf(L); }
    gemv_tile(T, pk2(dpp<0x00>(oval), dpp<0x55>(oval)), pk2(dpp<0xAA>(oval), dpp<0xFF>(oval)), red + 256 + w * 32, lane);
    __syncthreads();
    if (w == 0) {
      v4f a = {0.f, 0.f, 0.f, 0.f};
#pragma unroll
      for (int k = 0; k < 8; ++k) a += *(const v4f*)(red + 256 + k * 32 + 4 * (lane & 7));
      if (lane < 8) st128(rs, OFF_S4 + (g * 512 + 32 * j + 4 * lane) * 4, v4u{canon(f2u(a.x)), canon(f2u(a.y)), canon(f2u(a.z)), canon(f2u(a.w))});
      STAMP(9);
      if (j < 8) {
        const int c0 = 64 * g + 8 * j, hc = lane & 1, pr = lane >> 1;
        v4u s4; v2u ga, gb;
        const v2u xv = *(const GAS v2u*)(x + c0 + 4 * hc);
        for (u32 spin = 0; spin < SPIN_CAP; ++spin) {
          MEMBAR();
          s4 = ld128(rs, OFF_S4 + ((pr & 7) * 512 + c0 + 4 * hc) * 4);
          ga = __builtin_amdgcn_raw_buffer_load_b64(rs, OFF_MODR + (c0 + 4 * hc) * 2, 0, AUX_SC1);
          gb = __builtin_amdgcn_raw_buffer_load_b64(rs, OFF_MODR + (1536 + c0 + 4 * hc) * 2, 0, AUX_SC1);
          if (__all(max(umax3(mx4(s4), ga.x, ga.y), max(gb.x, gb.y)) != SENT)) break;
        }
        v4f a4 = __builtin_bit_cast(v4f, s4);
#pragma unroll
        for (int i = 0; i < 4; ++i) { float v = a4[i]; v += dpp<ROR8>(v); v += dpp<ROR4>(v); v += dpp<XOR2>(v); a4[i] = v; }
        float x1[4];
        x1[0] = rbf(lo2f(xv.x) + rbf(lo2f(ga.x) * rbf(a4.x))); x1[1] = rbf(hi2f(xv.x) + rbf(hi2f(ga.x) * rbf(a4.y)));
        x1[2] = rbf(lo2f(xv.y) + rbf(lo2f(ga.y) * rbf(a4.z))); x1[3] = rbf(hi2f(xv.y) + rbf(hi2f(ga.y) * rbf(a4.w)));
        STAMP(10);
        v4u f[4];
        for (u32 spin = 0; spin < SPIN_CAP; ++spin) {
          MEMBAR();
#pragma unroll
          for (int t = 0; t < 4; ++t) {
            const int p = 32 * t + pr;
            f[t] = ld128(rs, p < 112 ? OFF_S6 + (p * 512 + c0 + 4 * hc) * 4 : OOB);
          }
          if (__all(max(umax3(mx4(f[0]), mx4(f[1]), mx4(f[2])), mx4(f[3])) != SENT)) break;
        }
        STAMP(11);
        v4f s = (__builtin_bit_cast(v4f, f[0]) + __builtin_bit_cast(v4f, f[1])) + (__builtin_bit_cast(v4f, f[2]) + __builtin_bit_cast(v4f, f[3]));
#pragma unroll
        for (int i = 0; i < 4; ++i) { float v = s[i]; v += dpp<XOR2>(v); v += dpp<ROR4>(v); v += dpp<ROR8>(v); s[i] = xsum_rows(v); }
        const u32 o01 = pk2(x1[0] + rbf(lo2f(gb.x) * rbf(s.x)), x1[1] + rbf(hi2f(gb.x) * rbf(s.y)));
        const u32 o23 = pk2(x1[2] + rbf(lo2f(gb.y) * rbf(s.z)), x1[3] + rbf(hi2f(gb.y) * rbf(s.w)));
        if (lane < 2) *(GAS v2u*)(out + c0 + 4 * hc) = v2u{o01, o23};
        if (bid == 0 && lane == 0) st32(rs, OFF_EPOCH, tag);
        STAMP(12);
      }
    }
  } else {
    const int c = j;
    const int r16 = lane & 15, kg = lane >> 4;
    const size_t key0 = (size_t)g * SK + (SK - WIN) + 256 * c + 32 * w;
    v4u kf[2][4], vv[8];
#pragma unroll
    for (int tt = 0; tt < 2; ++tt)
#pragma unroll
      for (int s = 0; s < 4; ++s) kf[tt][s] = ldw(kc + (key0 + 16 * tt + r16) * DH + 32 * s + 8 * kg, NT_KV);
#pragma unroll
    for (int t = 0; t < 8; ++t) vv[t] = ldw(vc + (key0 + 16 * (t >> 2) + 4 * kg + (t & 3)) * DH + 8 * r16, NT_KV);
    STAMP(1);
    asm volatile("" : "+v"(tag)); tag += 1u;
    unsigned char* qs = lds + 4096;
    if (w < 2) {
      v4u q;
      for (u32 spin = 0; spin < SPIN_CAP; ++spin) {
        MEMBAR();
        q = ld128(rs, OFF_S2 + (g * 256 + 128 * w + 2 * lane) * 8);
        if (__all(((q.y ^ tag) | (q.w ^ tag)) == 0u)) break;
      }
      *(v2u*)(qs + (2 * w + (lane >> 5)) * 272 + (lane & 31) * 8) = v2u{q.x, q.z};
    }
    STAMP(2);
    v4u vt[8];
#pragma unroll
    for (int i = 0; i < 4; ++i) {
      vt[2 * i] = v4u{__builtin_amdgcn_perm(vv[1][i], vv[0][i], 0x05040100), __builtin_amdgcn_perm(vv[3][i], vv[2][i], 0x05040100),
                      __builtin_amdgcn_perm(vv[5][i], vv[4][i], 0x05040100), __builtin_amdgcn_perm(vv[7][i], vv[6][i], 0x05040100)};
      vt[2 * i + 1] = v4u{__builtin_amdgcn_perm(vv[1][i], vv[0][i], 0x07060302), __builtin_amdgcn_perm(vv[3][i], vv[2][i], 0x07060302),
                          __builtin_amdgcn_perm(vv[5][i], vv[4][i], 0x07060302), __builtin_amdgcn_perm(vv[7][i], vv[6][i], 0x07060302)};
    }
#pragma unroll
    for (int i = 0; i < 8; ++i) PIN4(vt[i]);
    __syncthreads();
    STAMP(3);
    v4f sc0 = {0.f, 0.f, 0.f, 0.f}, sc1 = {0.f, 0.f, 0.f, 0.f};
#pragma unroll
    for (int s = 0; s < 4; ++s) {
      const v4u qf = *(const v4u*)(qs + (lane & 3) * 272 + s * 64 + kg * 16);
      sc0 = mfma(kf[0][s], qf, sc0); sc1 = mfma(kf[1][s], qf, sc1);
    }
    float sv[8];
#pragma unroll
    for (int r = 0; r < 4; ++r) { sv[r] = rbf(rbf(sc0[r]) * 0.08838834764831845f); sv[4 + r] = rbf(rbf(sc1[r]) * 0.08838834764831845f); }
    float mw = fmaxf(fmaxf(fmaxf(sv[0], sv[1]), fmaxf(sv[2], sv[3])), fmaxf(fmaxf(sv[4], sv[5]), fmaxf(sv[6], sv[7])));
    mw = xmax_rows(mw);
    float e[8], lw = 0.f;
#pragma unroll
    for (int r = 0; r < 8; ++r) { e[r] = __builtin_amdgcn_exp2f((sv[r] - mw) * LOG2E); lw += e[r]; }
    lw = xsum_rows(lw);
    const v4u pf = {pk2(e[0], e[1]), pk2(e[2], e[3]), pk2(e[4], e[5]), pk2(e[6], e[7])};
    v4f o[8];
#pragma unroll
    for (int i = 0; i < 8; ++i) o[i] = mfma(vt[i], pf, v4f{0.f, 0.f, 0.f, 0.f});
    float* mo = (float*)(lds + 8192);
    float* mm = (float*)(lds + 6144);
    if (r16 < 4) {
      float* dst = mo + (w * 4 + r16) * 132 + 32 * kg;
#pragma unroll
      for (int r = 0; r < 4; ++r) {
        *(v4f*)(dst + 8 * r) = v4f{o[0][r], o[1][r], o[2][r], o[3][r]};
        *(v4f*)(dst + 8 * r + 4) = v4f{o[4][r], o[5][r], o[6][r], o[7][r]}; }
      if (kg == 0) { mm[r16 * 8 + w] = mw; mm[32 + r16 * 8 + w] = lw; }
    }
    STAMP(4);
    __syncthreads();
    { const int head = w >> 1;
      const v4f ma = *(const v4f*)(mm + head * 8), mb = *(const v4f*)(mm + head * 8 + 4);
      const v4f la = *(const v4f*)(mm + 32 + head * 8), lb = *(const v4f*)(mm + 32 + head * 8 + 4);
      const v4f pa = *(const v4f*)(mo + (kg * 4 + head) * 132 + 64 * (w & 1) + 4 * r16);
      const v4f pb = *(const v4f*)(mo + ((kg + 4) * 4 + head) * 132 + 64 * (w & 1) + 4 * r16);
      const float M = fmaxf(fmaxf(fmaxf(ma.x, ma.y), fmaxf(ma.z, ma.w)), fmaxf(fmaxf(mb.x, mb.y), fmaxf(mb.z, mb.w)));
      const float mA = kg == 0 ? ma.x : kg == 1 ? ma.y : kg == 2 ? ma.z : ma.w, mB = kg == 0 ? mb.x : kg == 1 ? mb.y : kg == 2 ? mb.z : mb.w;
      const float lA = kg == 0 ? la.x : kg == 1 ? la.y : kg == 2 ? la.z : la.w, lB = kg == 0 ? lb.x : kg == 1 ? lb.y : kg == 2 ? lb.z : lb.w;
      const float wA = __builtin_amdgcn_exp2f((mA - M) * LOG2E), wB = __builtin_amdgcn_exp2f((mB - M) * LOG2E);
      v4f s = wA * pa + wB * pb;
      s.x = xsum_rows(s.x); s.y = xsum_rows(s.y); s.z = xsum_rows(s.z); s.w = xsum_rows(s.w);
      const float L = xsum_rows(wA * lA + wB * lB);
      if (lane < 16) st128(rs, OFF_S3 + (((g * 16 + c) * 4 + head) * 128 + 64 * (w & 1) + 4 * lane) * 4,
                           v4u{canon(f2u(s.x)), canon(f2u(s.y)), canon(f2u(s.z)), canon(f2u(s.w))});
      if (lane == 0 && (w & 1) == 0) st64(rs, OFF_ML + ((g * 4 + head) * 16 + c) * 8, v2u{canon(f2u(M)), canon(f2u(L))});
    }
    STAMP(5);
    if (c < 14) {
      __syncthreads();
      const int f0 = 224 * g + 16 * c, cc = lane & 3, fi = g * 14 + c;
      v4u wt[4];
      const u16* wgu = (cc < 2 ? wg : wu) + f0 + 8 * (cc & 1);
#pragma unroll
      for (int t = 0; t < 4; ++t) wt[t] = ldw(wgu + (size_t)(64 * w + trow(lane, t)) * FF, NT_W);
      const int c8 = lane & 7, rp = lane >> 3;
      const v4u wd0 = ldw(wd + (size_t)(f0 + 2 * rp) * D + 64 * w + 8 * c8, NT_W);
      const v4u wd1 = ldw(wd + (size_t)(f0 + 2 * rp + 1) * D + 64 * w + 8 * c8, NT_W);
      const int col = 64 * w + tidx(lane);
      const float xv = lo2f(x[col]);
      float ga, sb, cb1;
      { u32 a, b, d;
        for (u32 spin = 0; spin < SPIN_CAP; ++spin) {
          MEMBAR();
          a = ld32(rs, OFF_MODR + (col >> 1) * 4); b = ld32(rs, OFF_MODR + ((512 + col) >> 1) * 4); d = ld32(rs, OFF_MODR + ((1024 + col) >> 1) * 4);
          if (__all(umax3(a, b, d) != SENT)) break;
        }
        const bool hi = col & 1;
        ga = hi ? hi2f(a) : lo2f(a); sb = hi ? hi2f(b) : lo2f(b); cb1 = rbf(1.f + (hi ? hi2f(d) : lo2f(d))); }
      const Tile48 T = pair_rows(wt);
      v4u dl = plo(wd0, wd1), dh = phi(wd0, wd1); PIN4(dl); PIN4(dh);
      STAMP(6);
      v4u pa, pb;
      for (u32 spin = 0; spin < SPIN_CAP; ++spin) {
        MEMBAR();
        pa = ld128(rs, OFF_S4 + (kg * 512 + 64 * w + 4 * r16) * 4);
        pb = ld128(rs, OFF_S4 + ((kg + 4) * 512 + 64 * w + 4 * r16) * 4);
        if (__all(max(mx4(pa), mx4(pb)) != SENT)) break;
      }
      STAMP(7);
      const float a = tr4(__builtin_bit_cast(v4f, pa) + __builtin_bit_cast(v4f, pb));
      const float x1 = rbf(xv + rbf(ga * rbf(a)));
      const float mean_w = wave_sum(x1) * (1.f / 64.f);
      const float dv = x1 - mean_w;
      const float m2_w = wave_sum(dv * dv);
      float* st = (float*)(lds + 2048);
      if (lane == 0) { st[w] = mean_w; st[8 + w] = m2_w; }
      __syncthreads();
      float h2;
      { const v4f ma = *(const v4f*)st, mb = *(const v4f*)(st + 4), qa = *(const v4f*)(st + 8), qb = *(const v4f*)(st + 12);
        const float mean = ((ma.x + ma.y) + (ma.z + ma.w) + (mb.x + mb.y) + (mb.z + mb.w)) * 0.125f;
        const v4f da = ma - mean, db = mb - mean;
        const float m2 = ((qa.x + qa.y) + (qa.z + qa.w) + (qb.x + qb.y) + (qb.z + qb.w)) +
                         64.f * ((da.x * da.x + da.y * da.y) + (da.z * da.z + da.w * da.w) + (db.x * db.x + db.y * db.y) + (db.z * db.z + db.w * db.w));
        const float den = rbf(__builtin_amdgcn_sqrtf(rbf(rbf(m2 * (1.f / D)) + 1e-5f)));
        const float lnv = rbf(rbf(x1 - rbf(mean)) * __builtin_amdgcn_rcpf(den));
        h2 = rbf(lnv * cb1) + sb; }
      gemv_tile(T, pk2(dpp<0x00>(h2), dpp<0x55>(h2)), pk2(dpp<0xAA>(h2), dpp<0xFF>(h2)), red + w * 32, lane);
      __syncthreads();
      STAMP(8);
      { const int fo = 2 * rp + (lane & 1);
        float gs = 0.f, us = 0.f;
#pragma unroll
        for (int k = 0; k < 8; ++k) { gs += red[k * 32 + fo]; us += red[k * 32 + 16 + fo]; }
        gs = rbf(gs); us = rbf(us);
        const float y2 = 2.f * 0.7978845608028654f * (gs + 0.044715f * gs * gs * gs);
        const float ge = rbf(gs * __builtin_amdgcn_rcpf(1.f + __builtin_amdgcn_exp2f(-y2 * LOG2E)));
        const float act = ge * us;
        const u32 ap = pk2(dpp<0xA0>(act), dpp<0xF5>(act));
        float d[8];
#pragma unroll
        for (int i = 0; i < 4; ++i) { d[2 * i] = dot2(dl[i], ap, 0.f); d[2 * i + 1] = dot2(dh[i], ap, 0.f); }
        v4f s = {tr32(d[0], d[4]), tr32(d[1], d[5]), tr32(d[2], d[6]), tr32(d[3], d[7])};
#pragma unroll
        for (int i = 0; i < 4; ++i) { float v = s[i]; v = tr16(v, v); v += dpp<ROR8>(v); s[i] = v; }
        if ((lane & 24) == 0) st128(rs, OFF_S6 + (fi * 512 + 64 * w + 8 * c8 + 4 * (lane >> 5)) * 4,
                                    v4u{canon(f2u(s.x)), canon(f2u(s.y)), canon(f2u(s.z)), canon(f2u(s.w))});
      }
      STAMP(9);
    }
  }
}

extern "C" void kernel_launch(void* const* d_in, const int* in_sizes, int n_in, void* d_out, int out_size, void* d_ws,
                              size_t ws_size, hipStream_t stream) {
  fused_block<<<256, 512, 0, stream>>>((const u16*)d_in[0], (const u16*)d_in[1], (const u16*)d_in[2], (const u16*)d_in[3],
                                       (const u16*)d_in[4], (const u16*)d_in[6], (const u16*)d_in[7], (const u16*)d_in[8],
                                       (const u16*)d_in[9], (const u16*)d_in[10], (u16*)d_out, (unsigned char*)d_ws);
}
```

```cpp
#include <hip/hip_runtime.h>
#include <stdint.h>

#define GAS __attribute__((address_space(1)))
#define LAS __attribute__((address_space(3)))
typedef unsigned short u16;
typedef unsigned u32;
typedef unsigned long long u64;
typedef u32 v4u __attribute__((ext_vector_type(4)));
typedef u32 v2u __attribute__((ext_vector_type(2)));
typedef float v4f __attribute__((ext_vector_type(4)));
typedef float v2f __attribute__((ext_vector_type(2)));
typedef __bf16 bf2 __attribute__((ext_vector_type(2)));
typedef __bf16 bf8 __attribute__((ext_vector_type(8)));
typedef __amdgpu_buffer_rsrc_t rsrc_t;

#define T2_MODE 1
#define T2_SLEEP 0
#define A_SLEEP 0
#define A_FIRST 8
#define ROLEMAP 0

constexpr int D = 512, SK = 65536, DH = 128, WIN = 4096, FF = 1792, NMOD = 3072;
constexpr int OFF_EPOCH = 0;
constexpr int OFF_S1 = 4096;
constexpr int OFF_S2 = OFF_S1 + 16384;
constexpr int OFF_MODR = OFF_S2 + 16384;
constexpr int OFF_S4 = OFF_MODR + 16384;
constexpr int OFF_FILL = 131072;
constexpr int OFF_S3 = OFF_FILL;
constexpr int OFF_S6 = OFF_S3 + 131072;
constexpr int OFF_ML = OFF_S6 + 114688;
constexpr int WS_BYTES = OFF_FILL + 262144;
static_assert(OFF_S4 + 16384 <= OFF_FILL && OFF_ML + 16384 == WS_BYTES, "workspace map");
constexpr int OOB = 0x7FFFFFF0;
constexpr u32 SENT = 0xFFFFFFFFu;
constexpr int AUX_SC1 = 16;
constexpr u32 SPIN_CAP = 1u << 17;
constexpr int LDS_QS = 0, LDS_MM = 2048, LDS_MO = 4096, LDS_K = 21504, LDS_BYTES = LDS_K + 65536;
static_assert(LDS_MO + 8 * 4 * 132 * 4 <= LDS_K, "LDS map");

#define ROLE_OF(idx) (((idx) & 1) ? 2 : (((idx) >> 1) & 1))
#define SUB_OF(idx) (((idx) & 1) ? (idx) >> 1 : (idx) >> 2)
#define STAMP(i) do {} while (0)

static __device__ __forceinline__ u32 f2u(float f) { return __builtin_bit_cast(u32, f); }
static __device__ __forceinline__ float u2f(u32 u) { return __builtin_bit_cast(float, u); }
static __device__ __forceinline__ float lo2f(u32 d) { return u2f(d << 16); }
static __device__ __forceinline__ float hi2f(u32 d) { return u2f(d & 0xffff0000u); }
static __device__ __forceinline__ u32 pk2(float lo, float hi) {
  v2f v = {lo, hi}; return __builtin_bit_cast(u32, __builtin_convertvector(v, bf2)); }
static __device__ __forceinline__ float rbf(float f) { return u2f(pk2(0.f, f)); }
static __device__ __forceinline__ float dot2(u32 a, u32 b, float c) {
  return __builtin_amdgcn_fdot2_f32_bf16(__builtin_bit_cast(bf2, a), __builtin_bit_cast(bf2, b), c, false); }
static __device__ __forceinline__ v4f mfma(v4u a, v4u b, v4f c) {
  return __builtin_amdgcn_mfma_f32_16x16x32_bf16(__builtin_bit_cast(bf8, a), __builtin_bit_cast(bf8, b), c, 0, 0, 0); }
static __device__ __forceinline__ u32 canon(u32 u) { return min(u, SENT - 1u); }
template <int CTRL> static __device__ __forceinline__ u32 dppu(u32 x) { return __builtin_amdgcn_mov_dpp(x, CTRL, 0xf, 0xf, true); }
template <int CTRL> static __device__ __forceinline__ float dpp(float x) { return u2f(dppu<CTRL>(f2u(x))); }
constexpr int XOR1 = 0xB1, XOR2 = 0x4E, HMIR = 0x141, ROR8 = 0x128, ROR4 = 0x124, SHR4 = 0x114, SHL4 = 0x104;
static __device__ __forceinline__ float tr32(float a, float b) {
  auto r = __builtin_amdgcn_permlane32_swap(f2u(a), f2u(b), false, false); return u2f(r[0]) + u2f(r[1]); }
static __device__ __forceinline__ float tr16(float a, float b) {
  auto r = __builtin_amdgcn_permlane16_swap(f2u(a), f2u(b), false, false); return u2f(r[0]) + u2f(r[1]); }
static __device__ __forceinline__ float xsum_rows(float x) { x = tr16(x, x); return tr32(x, x); }
static __device__ __forceinline__ float xmax_rows(float x) {
  auto r = __builtin_amdgcn_permlane16_swap(f2u(x), f2u(x), false, false); x = fmaxf(u2f(r[0]), u2f(r[1]));
  auto s = __builtin_amdgcn_permlane32_swap(f2u(x), f2u(x), false, false); return fmaxf(u2f(s[0]), u2f(s[1])); }
static __device__ __forceinline__ float wave_sum(float x) {
  x += dpp<XOR1>(x); x += dpp<XOR2>(x); x += dpp<HMIR>(x); x += dpp<ROR8>(x); return xsum_rows(x); }

static __device__ __forceinline__ v4u ldw(const u16* p) { return __builtin_nontemporal_load((const GAS v4u*)p); }
static __device__ __forceinline__ v4u ld128(rsrc_t rs, int off) { return __builtin_amdgcn_raw_buffer_load_b128(rs, off, 0, AUX_SC1); }
static __device__ __forceinline__ v2u ld64(rsrc_t rs, int off) { return __builtin_amdgcn_raw_buffer_load_b64(rs, off, 0, AUX_SC1); }
static __device__ __forceinline__ u32 ld32(rsrc_t rs, int off) { return __builtin_amdgcn_raw_buffer_load_b32(rs, off, 0, AUX_SC1); }
static __device__ __forceinline__ void st128(rsrc_t rs, int off, v4u v) { __builtin_amdgcn_raw_buffer_store_b128(v, rs, off, 0, AUX_SC1); }
static __device__ __forceinline__ void st64(rsrc_t rs, int off, v2u v) { __builtin_amdgcn_raw_buffer_store_b64(v, rs, off, 0, AUX_SC1); }
static __device__ __forceinline__ void st32(rsrc_t rs, int off, u32 v) { __builtin_amdgcn_raw_buffer_store_b32(v, rs, off, 0, AUX_SC1); }
static __device__ __forceinline__ u32 umax3(u32 a, u32 b, u32 c) { return max(max(a, b), c); }
static __device__ __forceinline__ u32 mx4(v4u v) { return max(umax3(v.x, v.y, v.z), v.w); }
#define MEMBAR() asm volatile("" ::: "memory")
#define PIN(v) asm volatile("" : "+v"(v))
#define POLL(LOADS, MISSING) do { bool miss_ = true; for (u32 spin_ = 0; spin_ < SPIN_CAP; ++spin_) { MEMBAR(); if (miss_) { LOADS; } miss_ = (MISSING); if (!__any(miss_)) break; } } while (0)

static __device__ __forceinline__ v4u plo(v4u a, v4u b) {
  return v4u{__builtin_amdgcn_perm(b.x, a.x, 0x05040100), __builtin_amdgcn_perm(b.y, a.y, 0x05040100),
             __builtin_amdgcn_perm(b.z, a.z, 0x05040100), __builtin_amdgcn_perm(b.w, a.w, 0x05040100)}; }
static __device__ __forceinline__ v4u phi(v4u a, v4u b) {
  return v4u{__builtin_amdgcn_perm(b.x, a.x, 0x07060302), __builtin_amdgcn_perm(b.y, a.y, 0x07060302),
             __builtin_amdgcn_perm(b.z, a.z, 0x07060302), __builtin_amdgcn_perm(b.w, a.w, 0x07060302)}; }
template <int NP> struct Tile { v4u lo[NP], hi[NP]; };
template <int NP> static __device__ __forceinline__ void pair_rows(const v4u (&w)[2 * NP], Tile<NP>& t) {
#pragma unroll
  for (int p = 0; p < NP; ++p) { t.lo[p] = plo(w[2 * p], w[2 * p + 1]); t.hi[p] = phi(w[2 * p], w[2 * p + 1]); PIN(t.lo[p]); PIN(t.hi[p]); } }
template <int NP> static __device__ __forceinline__ void dots(const Tile<NP>& t, const u32 (&a)[NP], float (&c)[8]) {
#pragma unroll
  for (int i = 0; i < 8; ++i) c[i] = 0.f;
#pragma unroll
  for (int p = 0; p < NP; ++p)
#pragma unroll
    for (int i = 0; i < 4; ++i) { c[2 * i] = dot2(t.lo[p][i], a[p], c[2 * i]); c[2 * i + 1] = dot2(t.hi[p][i], a[p], c[2 * i + 1]); }
}
static __device__ __forceinline__ float red_oct(const float (&c)[8], bool b3) {
  const float d0 = tr32(c[0], c[4]), d1 = tr32(c[1], c[5]), d2 = tr32(c[2], c[6]), d3 = tr32(c[3], c[7]);
  const float e0 = tr16(d0, d2), e1 = tr16(d1, d3);
  return (b3 ? e1 : e0) + dpp<ROR8>(b3 ? e0 : e1);
}
static __device__ __forceinline__ v2f red_quad(const float (&c)[8]) {
  const float d0 = tr32(c[0], c[4]), d1 = tr32(c[1], c[5]), d2 = tr32(c[2], c[6]), d3 = tr32(c[3], c[7]);
  float e0 = tr16(d0, d2), e1 = tr16(d1, d3);
  e0 += dpp<ROR4>(e0); e1 += dpp<ROR4>(e1); e0 += dpp<ROR8>(e0); e1 += dpp<ROR8>(e1);
  return v2f{e0, e1};
}
static __device__ __forceinline__ void oct_pairs(float v, u32 (&a)[4]) {
  const u32 p = pk2(v, dpp<XOR1>(v));
  const u32 lo = __builtin_amdgcn_update_dpp(p, p, SHR4, 0xf, 0xa, false), hi = __builtin_amdgcn_update_dpp(p, p, SHL4, 0xf, 0x5, false);
  a[0] = dppu<0x00>(lo); a[1] = dppu<0xAA>(lo); a[2] = dppu<0x00>(hi); a[3] = dppu<0xAA>(hi);
}
static __device__ __forceinline__ int tidx(int l) { return 4 * (l & 15) + 2 * (l >> 5) + ((l >> 4) & 1); }
static __device__ __forceinline__ float tr4(v4f v) { return tr16(tr32(v.x, v.z), tr32(v.y, v.w)); }
static __device__ __forceinline__ float silu_bf(float c) { return c * __builtin_amdgcn_rcpf(1.f + __builtin_amdgcn_exp2f(-1.4426950408889634f * c)); }

__global__ void __launch_bounds__(512, 2)
fused_block(const u16* __restrict__ x, const u16* __restrict__ kc, const u16* __restrict__ vc, const u16* __restrict__ wq,
            const u16* __restrict__ wo, const u16* __restrict__ wg, const u16* __restrict__ wu, const u16* __restrict__ wd,
            const u16* __restrict__ cnd, const u16* __restrict__ wmod, u16* __restrict__ out, unsigned char* ws) {
  __shared__ __attribute__((aligned(16))) unsigned char lds[LDS_BYTES];
  const int tid = threadIdx.x, lane = tid & 63, w = __builtin_amdgcn_readfirstlane(tid >> 6);
  const int bid = blockIdx.x, g = bid & 7, idx = bid >> 3, role = ROLE_OF(idx), sub = SUB_OF(idx);
  const rsrc_t rs = __builtin_amdgcn_make_buffer_rsrc(ws, 0, WS_BYTES, 0x00020000);
  STAMP(0);
  asm volatile("" :: "s"(x), "s"(kc), "s"(vc), "s"(wq), "s"(wo), "s"(wg), "s"(wu), "s"(wd), "s"(cnd), "s"(wmod), "s"(out));
  u32 tag = *(const __attribute__((address_space(4))) u32*)(ws + OFF_EPOCH);
#define TAG_READY() do { asm volatile("" : "+s"(tag)); tag += 1u; } while (0)
  float* red = (float*)lds;
  constexpr float LOG2E = 1.4426950408889634f;
  const int c8 = lane & 7, oct = lane >> 3;
  const bool b3 = (lane & 8) != 0;

  if (role == 2) {
    const int c = sub, r16 = lane & 15, kg = lane >> 4;
    const size_t key0 = (size_t)g * SK + (SK - WIN) + 256 * c + 32 * w;
    for (int i = 0; i < A_SLEEP; ++i) __builtin_amdgcn_s_sleep(2);
#pragma unroll
    for (int t = 0; t < 8; ++t) {
      if (t == A_FIRST) asm volatile("s_waitcnt vmcnt(0)" ::: "memory");
      __builtin_amdgcn_global_load_lds((const GAS u32*)(kc + (key0 + 4 * t + kg) * DH + 8 * (r16 ^ ((4 * t + kg) & 15))),
                                       (LAS u32*)(lds + LDS_K + w * 8192 + t * 1024), 16, 0, 2);
    }
    __builtin_amdgcn_s_barrier();
    v4u vv[8];
#pragma unroll
    for (int t = 0; t < 8; ++t) vv[t] = ldw(vc + (key0 + 16 * (t >> 2) + 4 * kg + (t & 3)) * DH + 8 * r16);
    STAMP(1);
    asm volatile("s_waitcnt vmcnt(8)" ::: "memory");
    STAMP(2);
    TAG_READY();
    v4u kf[2][4];
#pragma unroll
    for (int tt = 0; tt < 2; ++tt)
#pragma unroll
      for (int s = 0; s < 4; ++s) kf[tt][s] = *(const v4u*)(lds + LDS_K + w * 8192 + (16 * tt + r16) * 256 + (((4 * s + kg) ^ r16) * 16));
    asm volatile("" : "+v"(kf[0][0]), "+v"(kf[0][1]), "+v"(kf[0][2]), "+v"(kf[0][3]), "+v"(kf[1][0]), "+v"(kf[1][1]), "+v"(kf[1][2]), "+v"(kf[1][3]));
    unsigned char* qs = lds + LDS_QS;
    if (w < 2) {
      v4u q;
      POLL(q = ld128(rs, OFF_S2 + (g * 256 + 128 * w + 2 * lane) * 8), ((q.y ^ tag) | (q.w ^ tag)) != 0u);
      *(v2u*)(qs + (2 * w + (lane >> 5)) * 272 + (lane & 31) * 8) = v2u{q.x, q.z};
    }
    STAMP(3);
    v4u vt[8];
#pragma unroll
    for (int i = 0; i < 4; ++i) {
      vt[2 * i] = v4u{__builtin_amdgcn_perm(vv[1][i], vv[0][i], 0x05040100), __builtin_amdgcn_perm(vv[3][i], vv[2][i], 0x05040100),
                      __builtin_amdgcn_perm(vv[5][i], vv[4][i], 0x05040100), __builtin_amdgcn_perm(vv[7][i], vv[6][i], 0x05040100)};
      vt[2 * i + 1] = v4u{__builtin_amdgcn_perm(vv[1][i], vv[0][i], 0x07060302), __builtin_amdgcn_perm(vv[3][i], vv[2][i], 0x07060302),
                          __builtin_amdgcn_perm(vv[5][i], vv[4][i], 0x07060302), __builtin_amdgcn_perm(vv[7][i], vv[6][i], 0x07060302)};
    }
#pragma unroll
    for (int i = 0; i < 8; ++i) PIN(vt[i]);
    float* mo = (float*)(lds + LDS_MO);
    float* mm = (float*)(lds + LDS_MM);
    int s3off = OFF_S3 + (((g * 16 + c) * 4 + (w >> 1)) * 128 + 64 * (w & 1) + 32 * (r16 >> 3) + 8 * (2 * (lane >> 5) + (kg & 1)) + (r16 & 7)) * 2;
    if (lane & 1) s3off = OOB;
    PIN(s3off);
    STAMP(4);
    __syncthreads();
    STAMP(5);
    v4f sc0 = {0.f, 0.f, 0.f, 0.f}, sc1 = {0.f, 0.f, 0.f, 0.f};
#pragma unroll
    for (int s = 0; s < 4; ++s) {
      const v4u qf = *(const v4u*)(qs + (lane & 3) * 272 + s * 64 + kg * 16);
      sc0 = mfma(kf[0][s], qf, sc0); sc1 = mfma(kf[1][s], qf, sc1);
    }
    float sv[8];
#pragma unroll
    for (int r = 0; r < 4; ++r) { sv[r] = rbf(rbf(sc0[r]) * 0.08838834764831845f); sv[4 + r] = rbf(rbf(sc1[r]) * 0.08838834764831845f); }
    float mw = fmaxf(fmaxf(fmaxf(sv[0], sv[1]), fmaxf(sv[2], sv[3])), fmaxf(fmaxf(sv[4], sv[5]), fmaxf(sv[6], sv[7])));
    mw = xmax_rows(mw);
    float e[8];
#pragma unroll
    for (int r = 0; r < 8; ++r) e[r] = __builtin_amdgcn_exp2f((sv[r] - mw) * LOG2E);
    const v4u pf = {pk2(e[0], e[1]), pk2(e[2], e[3]), pk2(e[4], e[5]), pk2(e[6], e[7])};
    v4f o[8];
#pragma unroll
    for (int i = 0; i < 8; ++i) o[i] = mfma(vt[i], pf, v4f{0.f, 0.f, 0.f, 0.f});
    const float lw = xsum_rows(((e[0] + e[1]) + (e[2] + e[3])) + ((e[4] + e[5]) + (e[6] + e[7])));
    if (r16 < 4) {
      if (kg == 0) { mm[r16 * 8 + w] = mw; *(v2f*)(mm + 32 + (r16 * 8 + w) * 2) = v2f{mw, lw}; }
      float* dst = mo + (w * 4 + r16) * 132 + 32 * kg;
#pragma unroll
      for (int i = 0; i < 8; ++i) *(v4f*)(dst + 4 * i) = o[i];
    }
    STAMP(6);
    __syncthreads();
    { const int head = w >> 1;
      const v4f ma = *(const v4f*)(mm + head * 8), mb = *(const v4f*)(mm + head * 8 + 4);
      const float mA = mm[head * 8 + kg], mB = mm[head * 8 + kg + 4];
      const v4f pa = *(const v4f*)(mo + (kg * 4 + head) * 132 + 64 * (w & 1) + 4 * r16);
      const v4f pb = *(const v4f*)(mo + ((kg + 4) * 4 + head) * 132 + 64 * (w & 1) + 4 * r16);
      const float M = fmaxf(fmaxf(fmaxf(ma.x, ma.y), fmaxf(ma.z, ma.w)), fmaxf(fmaxf(mb.x, mb.y), fmaxf(mb.z, mb.w)));
      const float wA = __builtin_amdgcn_exp2f((mA - M) * LOG2E), wB = __builtin_amdgcn_exp2f((mB - M) * LOG2E);
      const float v = tr4(wA * pa + wB * pb);
      st32(rs, s3off, canon(pk2(v, dpp<XOR1>(v))));
      if (w == 1) {
        const v2f ml = *(const v2f*)(mm + 32 + (lane & 31) * 2);
        float Mx = fmaxf(ml.x, dpp<XOR1>(ml.x)); Mx = fmaxf(Mx, dpp<XOR2>(Mx)); Mx = fmaxf(Mx, dpp<HMIR>(Mx));
        float L = ml.y * __builtin_amdgcn_exp2f((ml.x - Mx) * LOG2E);
        L += dpp<XOR1>(L); L += dpp<XOR2>(L); L += dpp<HMIR>(L);
        st64(rs, (lane & 39) == 0 ? OFF_ML + (g * 16 + c) * 128 + lane : OOB, v2u{canon(f2u(Mx)), canon(f2u(L))});
      }
    }
    STAMP(7);
  } else if (role == 1) {
    const int jj = sub;
    const int rho = 2 * (lane & 31) + (lane >> 5);
    v4u wt[8];
#pragma unroll
    for (int t = 0; t < 8; ++t) { const int l = 8 * oct + t; wt[t] = ldw(wq + (size_t)(64 * w + 2 * (l & 31) + (l >> 5)) * 4096 + 512 * g + 64 * jj + 8 * c8); }
    const v4u xall = *(const GAS v4u*)(x + 8 * lane);
    const u32 xown = x[64 * w + rho];
    STAMP(1);
    float ln;
    { float s = 0.f;
#pragma unroll
      for (int i = 0; i < 4; ++i) s += lo2f(xall[i]) + hi2f(xall[i]);
      const float mean = wave_sum(s) * (1.f / D);
      float v = 0.f;
#pragma unroll
      for (int i = 0; i < 4; ++i) { const float a = lo2f(xall[i]) - mean, b = hi2f(xall[i]) - mean; v += a * a + b * b; }
      const float var = rbf(wave_sum(v) * (1.f / D));
      const float den = rbf(sqrtf(rbf(var + 1e-5f)));
      ln = rbf(rbf(lo2f(xown) - rbf(mean)) / den); PIN(ln); }
    Tile<4> T; pair_rows<4>(wt, T);
    TAG_READY();
    int s1off = OFF_S1 + ((lane >> 5) * 512 + 64 * w + 2 * (lane & 31)) * 8; PIN(s1off);
    STAMP(2);
    v4u p0, p1;
    POLL(p0 = ld128(rs, s1off); p1 = ld128(rs, s1off + 8192), ((p0.y ^ tag) | (p0.w ^ tag) | (p1.y ^ tag) | (p1.w ^ tag)) != 0u);
    STAMP(3);
    { const float ev = rbf(u2f(p0.x) + u2f(p1.x)), od = rbf(u2f(p0.z) + u2f(p1.z));
      auto r = __builtin_amdgcn_permlane32_swap(f2u(ev), f2u(od), false, false);
      const float h = rbf(ln * rbf(1.f + u2f(r[1]))) + u2f(r[0]);
      u32 a[4]; oct_pairs(h, a);
      float cs[8]; dots<4>(T, a, cs);
      red[w * 64 + 8 * c8 + oct] = red_oct(cs, b3); }
    __syncthreads();
    if (w == 0) {
      float a = 0.f;
#pragma unroll
      for (int k = 0; k < 8; ++k) a += red[k * 64 + lane];
      st64(rs, (lane & 1) ? OOB : OFF_S2 + (g * 256 + 32 * jj) * 8 + lane * 4, v2u{pk2(a, dpp<XOR1>(a)), tag});
    }
    STAMP(4);
    if (jj < 7) {
      __syncthreads();
      const int f0 = 224 * g + 32 * jj, fi = g * 7 + jj, kg = lane >> 4, r16 = lane & 15;
      const u16* wgu = (c8 < 4 ? wg : wu) + f0 + 8 * (c8 & 3);
#pragma unroll
      for (int t = 0; t < 8; ++t) wt[t] = ldw(wgu + (size_t)(64 * w + tidx(8 * oct + t)) * FF);
      v4u wdn[4];
#pragma unroll
      for (int t = 0; t < 4; ++t) wdn[t] = ldw(wd + (size_t)(f0 + 4 * oct + t) * D + 64 * w + 8 * c8);
      const int col = 64 * w + tidx(lane);
      float xv = lo2f(x[col]);
      float ga, sb, cb1;
      { u32 a0, b0, d0, a1, b1, d1; const int mo = OFF_MODR + col * 4;
        POLL(a0 = ld32(rs, mo); b0 = ld32(rs, mo + 2048); d0 = ld32(rs, mo + 4096); a1 = ld32(rs, mo + 8192); b1 = ld32(rs, mo + 10240); d1 = ld32(rs, mo + 12288),
             max(umax3(a0, b0, d0), umax3(a1, b1, d1)) == SENT);
        ga = rbf(u2f(a0) + u2f(a1)); sb = rbf(u2f(b0) + u2f(b1)); cb1 = rbf(1.f + rbf(u2f(d0) + u2f(d1))); }
      pair_rows<4>(wt, T);
      Tile<2> TD; pair_rows<2>(wdn, TD);
      int s4off = OFF_S4 + (kg * 512 + 64 * w + 4 * r16) * 4, s6off = OFF_S6 + (fi * 512 + 64 * w + 8 * c8 + oct) * 4;
      PIN(xv); PIN(ga); PIN(sb); PIN(cb1); PIN(s4off); PIN(s6off);
      STAMP(5);
      v4u pa, pb;
      POLL(pa = ld128(rs, s4off); pb = ld128(rs, s4off + 8192), max(mx4(pa), mx4(pb)) == SENT);
      STAMP(6);
      const float a = tr4(__builtin_bit_cast(v4f, pa) + __builtin_bit_cast(v4f, pb));
      const float x1 = rbf(xv + rbf(ga * rbf(a)));
      float* st = red + 1024;
      { const float s1 = wave_sum(x1), s2 = wave_sum(x1 * x1);
        if (lane == 0) *(v2f*)(st + 2 * w) = v2f{s1, s2}; }
      __syncthreads();
      { const v4f q0 = *(const v4f*)st, q1 = *(const v4f*)(st + 4), q2 = *(const v4f*)(st + 8), q3 = *(const v4f*)(st + 12);
        const v4f q = (q0 + q1) + (q2 + q3);
        const float mean = (q.x + q.z) * (1.f / D);
        const float var = fmaxf((q.y + q.w) * (1.f / D) - mean * mean, 0.f);
        const float den = rbf(__builtin_amdgcn_sqrtf(rbf(rbf(var) + 1e-5f)));
        const float lnv = rbf(rbf(x1 - rbf(mean)) * __builtin_amdgcn_rcpf(den));
        const float h2 = rbf(lnv * cb1) + sb;
        u32 ap[4]; oct_pairs(h2, ap);
        float cs[8]; dots<4>(T, ap, cs);
        red[512 + w * 64 + 2 * (8 * (c8 & 3) + oct) + (c8 >> 2)] = red_oct(cs, b3); }
      __syncthreads();
      STAMP(7);
      { const int fo = 4 * oct + (lane & 3);
        v2f gu[8];
#pragma unroll
        for (int k = 0; k < 8; ++k) gu[k] = *(const v2f*)(red + 512 + k * 64 + 2 * fo);
        const v2f sgu = ((gu[0] + gu[1]) + (gu[2] + gu[3])) + ((gu[4] + gu[5]) + (gu[6] + gu[7]));
        const float gs = rbf(sgu.x), us = rbf(sgu.y);
        const float z = gs * (-2.f * 0.7978845608028654f * LOG2E) * __builtin_fmaf(0.044715f * gs, gs, 1.f);
        const float act = rbf(gs * __builtin_amdgcn_rcpf(1.f + __builtin_amdgcn_exp2f(z))) * us;
        const u32 p = pk2(act, dpp<XOR1>(act));
        const u32 ap[2] = {dppu<0x00>(p), dppu<0xAA>(p)};
        float cs[8]; dots<2>(TD, ap, cs);
        st32(rs, s6off, canon(f2u(red_oct(cs, b3)))); }
      STAMP(8);
    }
  } else {
    const int jj = sub, ti = g * 8 + jj;
    const int cc = lane & 3, rb = lane >> 2, cg = 4 * g + (jj & 3), half = jj >> 2;
    if (w < 5) {
      int off = OFF_FILL + ti * 4096 + tid * 16;
      if (w == 4) off = lane < 16 ? OFF_S4 + (g * 512 + 64 * jj) * 4 + lane * 16 : lane < 32 ? OFF_MODR + (half * 2048 + 64 * cg) * 4 + (lane - 16) * 16 : OOB;
      st128(rs, off, v4u{SENT, SENT, SENT, SENT});
    }
    MEMBAR();
    const int r0 = 256 * half + 32 * w + 2 * rb;
    const u32 cp = *(const GAS u32*)(cnd + r0);
    v4u wm[2];
    wm[0] = ldw(wmod + (size_t)r0 * NMOD + 32 * cg + 8 * cc);
    wm[1] = ldw(wmod + (size_t)(r0 + 1) * NMOD + 32 * cg + 8 * cc);
    const v2u cq = *(const GAS v2u*)(cnd + 256 * half + 32 * w + 4 * oct);
    STAMP(1);
    { const u32 a[1] = {pk2(silu_bf(lo2f(cp)), silu_bf(hi2f(cp)))};
      Tile<1> TM; pair_rows<1>(wm, TM);
      float cs[8]; dots<1>(TM, a, cs);
      const v2f r = red_quad(cs);
      TAG_READY();
      asm volatile("s_waitcnt vmcnt(1)" ::: "memory");
      if ((lane & 12) == 0) *(v2f*)(red + w * 32 + 8 * cc + 2 * ((lane >> 4) & 1) + 4 * (lane >> 5)) = r; }
    STAMP(2);
    __syncthreads();
    if (w == 0) {
      float a = 0.f;
#pragma unroll
      for (int k = 0; k < 8; ++k) a += red[k * 32 + (lane & 31)];
      st64(rs, lane < 32 ? OFF_S1 + (half * 1024 + 32 * cg + lane) * 8 : OOB, v2u{f2u(a), tag});
    }
    STAMP(3);
    if (w == 0) {
      u32 tg;
      POLL(tg = ld32(rs, OFF_S1 + lane * 256 + 4), tg != tag);
      POLL(tg = ld32(rs, OFF_S2 + g * 2048 + (lane & 7) * 256 + 4), tg != tag);
      for (int i = 0; i < T2_SLEEP; ++i) __builtin_amdgcn_s_sleep(2);
    }
    __syncthreads();
    STAMP(4);
    v4u wr[4];
#pragma unroll
    for (int t = 0; t < 4; ++t) wr[t] = ldw(wmod + (size_t)(256 * half + 32 * w + 4 * oct + t) * NMOD + 1024 + 64 * cg + 8 * c8);
    v4u wt[8];
#pragma unroll
    for (int t = 0; t < 8; ++t) wt[t] = ldw(wo + (size_t)(512 * g + 64 * w + 8 * t + oct) * D + 64 * jj + 8 * c8);
    { const u32 a[2] = {pk2(silu_bf(lo2f(cq.x)), silu_bf(hi2f(cq.x))), pk2(silu_bf(lo2f(cq.y)), silu_bf(hi2f(cq.y)))};
      Tile<2> TR; pair_rows<2>(wr, TR);
      float cs[8]; dots<2>(TR, a, cs);
      red[1024 + w * 64 + 8 * c8 + oct] = red_oct(cs, b3); }
    __syncthreads();
    if (w == 0) {
      float a = 0.f;
#pragma unroll
      for (int k = 0; k < 8; ++k) a += red[1024 + k * 64 + lane];
      st32(rs, OFF_MODR + (half * 2048 + 64 * cg + lane) * 4, canon(f2u(a)));
    }
    Tile<4> T; pair_rows<4>(wt, T);
    int s3off = OFF_S3 + (((g * 16 + oct) * 4 + (w >> 1)) * 128 + 64 * (w & 1) + 8 * c8) * 2, mloff = OFF_ML + (g * 16 + oct) * 128 + (w >> 1) * 8;
    PIN(s3off); PIN(mloff);
    STAMP(5);
    { v4u oa, ob; v2u ma, mb;
      POLL(ma = ld64(rs, mloff); mb = ld64(rs, mloff + 1024); oa = ld128(rs, s3off); ob = ld128(rs, s3off + 8192),
           max(umax3(mx4(oa), mx4(ob), max(ma.x, ma.y)), max(mb.x, mb.y)) == SENT);
      STAMP(6);
      float M = fmaxf(u2f(ma.x), u2f(mb.x)); M = xmax_rows(fmaxf(M, dpp<ROR8>(M)));
      const float wa = __builtin_amdgcn_exp2f((u2f(ma.x) - M) * LOG2E), wb = __builtin_amdgcn_exp2f((u2f(mb.x) - M) * LOG2E);
      float L = wa * u2f(ma.y) + wb * u2f(mb.y); L = xsum_rows(L + dpp<ROR8>(L));
      float cs[8];
#pragma unroll
      for (int i = 0; i < 4; ++i) { cs[2 * i] = wa * lo2f(oa[i]) + wb * lo2f(ob[i]); cs[2 * i + 1] = wa * hi2f(oa[i]) + wb * hi2f(ob[i]); }
      const float ov = red_oct(cs, b3) * __builtin_amdgcn_rcpf(L);
      u32 a[4]; oct_pairs(ov, a);
      dots<4>(T, a, cs);
      red[512 + w * 64 + 8 * c8 + oct] = red_oct(cs, b3); }
    __syncthreads();
    if (w == 0) {
      { float a = 0.f;
#pragma unroll
        for (int k = 0; k < 8; ++k) a += red[512 + k * 64 + lane];
        st32(rs, OFF_S4 + (g * 512 + 64 * jj + lane) * 4, canon(f2u(a))); }
      STAMP(7);
      const int c0 = 64 * g + 8 * jj, hc = lane & 1, pr = lane >> 1;
      const int col = c0 + 4 * hc + 2 * (lane >> 5) + ((lane >> 4) & 1);
      const float xv = lo2f(x[col]);
      float x1, gb;
      { v4u s4; u32 a0, a1, q0, q1; const int mo = OFF_MODR + col * 4;
        POLL(s4 = ld128(rs, OFF_S4 + ((pr & 7) * 512 + c0 + 4 * hc) * 4); a0 = ld32(rs, mo); q0 = ld32(rs, mo + 6144); a1 = ld32(rs, mo + 8192); q1 = ld32(rs, mo + 14336),
             max(umax3(mx4(s4), a0, a1), max(q0, q1)) == SENT);
        const bool b4 = lane & 16, b5 = lane & 32;
        float v = u2f(b5 ? (b4 ? s4.w : s4.z) : (b4 ? s4.y : s4.x));
        v += dpp<ROR8>(v); v += dpp<ROR4>(v); v += dpp<XOR2>(v);
        x1 = rbf(xv + rbf(rbf(u2f(a0) + u2f(a1)) * rbf(v)));
        gb = rbf(u2f(q0) + u2f(q1)); }
      int f0off = OFF_S6 + (pr * 512 + c0 + 4 * hc) * 4, f1off = pr < 24 ? f0off + 65536 : OOB;
      PIN(x1); PIN(gb); PIN(f0off); PIN(f1off);
      STAMP(8);
      v4u f0, f1;
      POLL(f0 = ld128(rs, f0off); f1 = ld128(rs, f1off), max(mx4(f0), mx4(f1)) == SENT);
      STAMP(9);
      float s = tr4(__builtin_bit_cast(v4f, f0) + __builtin_bit_cast(v4f, f1));
      s += dpp<ROR8>(s); s += dpp<ROR4>(s); s += dpp<XOR2>(s);
      const u32 ob = pk2(x1 + rbf(gb * rbf(s)), 0.f);
      if ((lane & 14) == 0) __hip_atomic_store((GAS u16*)(out + col), (u16)ob, __ATOMIC_RELAXED, __HIP_MEMORY_SCOPE_AGENT);
      if (ti == 0 && lane == 0) st32(rs, OFF_EPOCH, tag);
      STAMP(10);
    }
  }
}

extern "C" void kernel_launch(void* const* d_in, const int* in_sizes, int n_in, void* d_out, int out_size, void* d_ws,
                              size_t ws_size, hipStream_t stream) {
  fused_block<<<256, 512, 0, stream>>>((const u16*)d_in[0], (const u16*)d_in[1], (const u16*)d_in[2], (const u16*)d_in[3],
                                       (const u16*)d_in[4], (const u16*)d_in[6], (const u16*)d_in[7], (const u16*)d_in[8],
                                       (const u16*)d_in[9], (const u16*)d_in[10], (u16*)d_out, (unsigned char*)d_ws);
}
```

```cpp
#include <hip/hip_runtime.h>
#include <stdint.h>
typedef unsigned short u16;
__global__ void __launch_bounds__(320, 2)
fused_block(const u16* x, const u16* kc, const u16* vc, const u16* wq, const u16* wo, const u16* wg, const u16* wu, const u16* wd,
            const u16* cnd, const u16* wmod, u16* out, unsigned char* ws) {
#if defined(__HIP_DEVICE_COMPILE__)
  __shared__ __attribute__((aligned(16))) unsigned char lds[147456];
  auto ka = __builtin_amdgcn_kernarg_segment_ptr();
  unsigned bid = blockIdx.x, tid = threadIdx.x;
  asm volatile(
    ".Lfb_begin:\n"
    "s_load_dword s90, s[0:1], 0x0\n"
    "s_load_dword s91, s[0:1], 0x40\n"
    "s_getpc_b64 s[92:93]\n"
    "s_and_b32 s92, s92, 0xffffff00\n"
    "v_readfirstlane_b32 s3, v0\n"
    "s_cmp_gt_u32 s3, 0xff\n"
    "s_cbranch_scc1 .Lfb_helpers\n"
    "s_cmp_gt_u32 s2, 127\n"
    "s_cbranch_scc1 .Lfb_role_a\n"
    "s_cmp_lt_u32 s2, 64\n"
    "s_cbranch_scc1 .Lfb_role_q\n"
    "s_and_b32 s10, s2, 7\n"
    "s_ashr_i32 s2, s2, 3\n"
    "v_bfe_u32 v2, v0, 4, 2\n"
    "s_load_dwordx4 s[20:23], s[0:1], 0x40\n"
    "v_readfirstlane_b32 s3, v0\n"
    "s_add_i32 s17, s2, -8\n"
    "v_lshlrev_b32_e32 v1, 2, v2\n"
    "s_lshr_b32 s12, s3, 6\n"
    "v_lshl_or_b32 v1, s17, 6, v1\n"
    "v_and_b32_e32 v5, 15, v0\n"
    "v_lshl_add_u32 v1, s12, 4, v1\n"
    "s_movk_i32 s18, 0x1800\n"
    "v_mul_lo_u32 v6, v1, s18\n"
    "s_lshl_b32 s3, s10, 8\n"
    "v_lshlrev_b32_e32 v4, 4, v5\n"
    "v_lshlrev_b32_e32 v1, 1, v1\n"
    "s_waitcnt lgkmcnt(0)\n"
    "global_load_dwordx2 v[22:23], v1, s[20:21]\n"
    "s_and_b32 s5, s23, 0xffff\n"
    "s_mov_b32 s7, 0x20000\n"
    "s_mov_b32 s6, 0x300000\n"
    "s_mov_b32 s4, s22\n"
    "v_or3_b32 v18, v6, s3, v4\n"
    "s_mov_b32 s19, 0\n"
    "buffer_load_dwordx4 v[6:9], v18, s[4:7], s19 offen nt\n"
    "buffer_load_dwordx4 v[10:13], v18, s[4:7], s18 offen nt\n"
    "s_movk_i32 s3, 0x3000\n"
    "buffer_load_dwordx4 v[14:17], v18, s[4:7], s3 offen nt\n"
    "s_movk_i32 s3, 0x4800\n"
    "buffer_load_dwordx4 v[18:21], v18, s[4:7], s3 offen nt\n"
    "v_and_b32_e32 v3, 63, v0\n"
    "v_mov_b32_e32 v103, 0\n"
    "v_bfe_u32 v1, v0, 3, 3\n"
    "s_branch .Lfb_role_b_tail\n"
    ".Lfb_helpers:\n"
    "s_mov_b32 s94, .Lfb_end-.Lfb_begin\n"
    "s_cmp_gt_u32 s2, 127\n"
    "s_cbranch_scc1 .Lfb_role_ah\n"
    "s_cmp_lt_u32 s2, 64\n"
    "s_cbranch_scc1 .Lfb_role_qh\n"
    "s_branch .Lfb_role_bh\n"
    ".Lfb_role_q:\n"
    "v_readfirstlane_b32 s13, v0\n"
    "s_load_dwordx2 s[8:9], s[0:1], 0x0\n"
    "s_load_dwordx2 s[4:5], s[0:1], 0x18\n"
    "s_lshr_b32 s19, s13, 6\n"
    "s_bfe_u32 s20, s2, 0x20003\n"
    "s_lshl_b32 s14, s20, 7\n"
    "s_lshl_b32 s21, s19, 5\n"
    "s_and_b32 s12, s2, 7\n"
    "s_ashr_i32 s17, s2, 5\n"
    "s_add_i32 s2, s21, s14\n"
    "v_bfe_u32 v70, v0, 5, 1\n"
    "v_and_b32_e32 v71, 31, v0\n"
    "v_or_b32_e32 v2, s2, v70\n"
    "v_lshlrev_b32_e32 v3, 4, v71\n"
    "v_lshlrev_b32_e32 v2, 13, v2\n"
    "s_lshl_b32 s3, s17, 9\n"
    "v_lshl_or_b32 v3, s12, 10, v3\n"
    "v_and_b32_e32 v1, 63, v0\n"
    "s_waitcnt lgkmcnt(0)\n"
    "s_and_b32 s5, s5, 0xffff\n"
    "s_mov_b32 s7, 0x20000\n"
    "s_mov_b32 s6, 0x400000\n"
    "v_add3_u32 v6, v3, s3, v2\n"
    "s_mov_b32 s15, 0\n"
    "buffer_load_dwordx4 v[58:61], v6, s[4:7], s15 offen nt\n"
    "s_movk_i32 s3, 0x4000\n"
    "buffer_load_dwordx4 v[62:65], v6, s[4:7], s3 offen nt\n"
    "v_lshlrev_b32_e32 v1, 4, v1\n"
    "v_lshlrev_b32_e32 v72, 1, v0\n"
    "global_load_dwordx4 v[66:69], v1, s[8:9]\n"
    "v_and_b32_e32 v1, 30, v72\n"
    "v_or3_b32 v1, v1, v70, s2\n"
    "v_lshlrev_b32_e32 v1, 1, v1\n"
    "global_load_ushort v73, v1, s[8:9]\n"
    "s_mov_b32 s18, 0x8000\n"
    "buffer_load_dwordx4 v[50:53], v6, s[4:7], s18 offen nt\n"
    "s_mov_b32 s2, 0xc000\n"
    "buffer_load_dwordx4 v[54:57], v6, s[4:7], s2 offen nt\n"
    "s_mov_b32 s2, 0x10000\n"
    "buffer_load_dwordx4 v[42:45], v6, s[4:7], s2 offen nt\n"
    "s_mov_b32 s2, 0x14000\n"
    "buffer_load_dwordx4 v[46:49], v6, s[4:7], s2 offen nt\n"
    "s_mov_b32 s2, 0x18000\n"
    "buffer_load_dwordx4 v[34:37], v6, s[4:7], s2 offen nt\n"
    "s_mov_b32 s2, 0x1c000\n"
    "buffer_load_dwordx4 v[38:41], v6, s[4:7], s2 offen nt\n"
    "buffer_load_dwordx4 v[26:29], v6, s[4:7], s7 offen nt\n"
    "s_mov_b32 s2, 0x24000\n"
    "buffer_load_dwordx4 v[30:33], v6, s[4:7], s2 offen nt\n"
    "s_mov_b32 s2, 0x28000\n"
    "buffer_load_dwordx4 v[18:21], v6, s[4:7], s2 offen nt\n"
    "s_mov_b32 s2, 0x2c000\n"
    "buffer_load_dwordx4 v[22:25], v6, s[4:7], s2 offen nt\n"
    "s_mov_b32 s2, 0x30000\n"
    "buffer_load_dwordx4 v[10:13], v6, s[4:7], s2 offen nt\n"
    "s_mov_b32 s2, 0x34000\n"
    "buffer_load_dwordx4 v[14:17], v6, s[4:7], s2 offen nt\n"
    "s_mov_b32 s2, 0x38000\n"
    "buffer_load_dwordx4 v[2:5], v6, s[4:7], s2 offen nt\n"
    "s_mov_b32 s2, 0x3c000\n"
    "buffer_load_dwordx4 v[6:9], v6, s[4:7], s2 offen nt\n"
    "v_and_b32_e32 v1, 7, v0\n"
    "s_branch .Lfb_role_q_tail\n"
    ".Lfb_role_a:\n"
    "s_ashr_i32 s8, s2, 3\n"
    "s_and_b32 s4, s2, 7\n"
    "s_add_i32 s11, s8, -16\n"
    "s_mul_i32 s10, s4, 0x9800\n"
    "s_lshl_b32 s2, s11, 16\n"
    "s_lshl_b32 s4, s4, 24\n"
    "s_load_dwordx4 s[12:15], s[0:1], 0x8\n"
    "v_readfirstlane_b32 s3, v0\n"
    "s_add_i32 s2, s2, s4\n"
    "s_lshl_b32 s4, s8, 11\n"
    "s_lshr_b32 s9, s3, 6\n"
    "v_bfe_u32 v3, v0, 4, 2\n"
    "s_and_b32 s4, s4, 0x3000\n"
    "v_and_b32_e32 v2, 15, v0\n"
    "s_lshl_b32 s16, s9, 14\n"
    "s_or_b32 s2, s2, s4\n"
    "v_bitop3_b32 v5, v3, v0, 15 bitop3:0x78\n"
    "v_lshlrev_b32_e32 v4, 8, v3\n"
    "v_lshlrev_b32_e32 v5, 4, v5\n"
    "v_lshlrev_b32_e32 v8, 6, v3\n"
    "v_lshlrev_b32_e32 v9, 4, v2\n"
    "s_movk_i32 s18, 0x80\n"
    "s_movk_i32 s19, 0xc0\n"
    "s_add_i32 s2, s2, s16\n"
    "s_add_i32 s17, s16, 0x4000\n"
    "v_or_b32_e32 v7, v5, v4\n"
    "v_xor_b32_e32 v10, v8, v9\n"
    "v_bitop3_b32 v8, v8, v4, v9 bitop3:0xde\n"
    "v_bitop3_b32 v9, v5, 64, v4 bitop3:0x36\n"
    "v_bitop3_b32 v11, v5, s18, v4 bitop3:0x36\n"
    "v_bitop3_b32 v12, v5, s19, v4 bitop3:0x36\n"
    "s_waitcnt lgkmcnt(0)\n"
    "s_and_b32 s5, s13, 0xffff\n"
    "s_mov_b32 s7, 0x20000\n"
    "s_brev_b32 s6, 16\n"
    "s_mov_b32 s4, s12\n"
    "s_sleep 8\n"
    "s_add_i32 s12, s2, 0xf00000\n"
    "s_mov_b32 m0, s17\n"
    "s_nop 0\n"
    "buffer_load_dwordx4 v7, s[4:7], s12 offen nt lds\n"
    "buffer_load_dwordx4 v9, s[4:7], s12 offen offset:1024 nt lds\n"
    "buffer_load_dwordx4 v11, s[4:7], s12 offen offset:2048 nt lds\n"
    "buffer_load_dwordx4 v12, s[4:7], s12 offen offset:3072 nt lds\n"
    "s_add_i32 s2, s16, 0x5000\n"
    "s_xor_b32 s20, s12, 0x1000\n"
    "s_mov_b32 m0, s2\n"
    "s_nop 0\n"
    "buffer_load_dwordx4 v7, s[4:7], s20 offen nt lds\n"
    "buffer_load_dwordx4 v9, s[4:7], s20 offen offset:1024 nt lds\n"
    "buffer_load_dwordx4 v11, s[4:7], s20 offen offset:2048 nt lds\n"
    "buffer_load_dwordx4 v12, s[4:7], s20 offen offset:3072 nt lds\n"
    "s_add_i32 s21, s16, 0x6000\n"
    "s_xor_b32 s22, s12, 0x2000\n"
    "s_mov_b32 m0, s21\n"
    "s_nop 0\n"
    "buffer_load_dwordx4 v7, s[4:7], s22 offen nt lds\n"
    "buffer_load_dwordx4 v9, s[4:7], s22 offen offset:1024 nt lds\n"
    "buffer_load_dwordx4 v11, s[4:7], s22 offen offset:2048 nt lds\n"
    "buffer_load_dwordx4 v12, s[4:7], s22 offen offset:3072 nt lds\n"
    "s_add_i32 s21, s16, 0x7000\n"
    "s_xor_b32 s23, s12, 0x3000\n"
    "s_mov_b32 m0, s21\n"
    "s_nop 0\n"
    "buffer_load_dwordx4 v7, s[4:7], s23 offen nt lds\n"
    "buffer_load_dwordx4 v9, s[4:7], s23 offen offset:1024 nt lds\n"
    "buffer_load_dwordx4 v11, s[4:7], s23 offen offset:2048 nt lds\n"
    "buffer_load_dwordx4 v12, s[4:7], s23 offen offset:3072 nt lds\n"
    "v_bitop3_b32 v13, v10, 32, v4 bitop3:0x36\n"
    "s_add_i32 s21, s16, 0x14000\n"
    "v_bitop3_b32 v9, v10, 16, v4 bitop3:0x36\n"
    "v_bitop3_b32 v4, v10, 48, v4 bitop3:0x36\n"
    "s_and_b32 s5, s15, 0xffff\n"
    "s_mov_b32 s4, s14\n"
    "s_mov_b32 m0, s21\n"
    "s_nop 0\n"
    "buffer_load_dwordx4 v8, s[4:7], s12 offen nt lds\n"
    "buffer_load_dwordx4 v9, s[4:7], s12 offen offset:1024 nt lds\n"
    "buffer_load_dwordx4 v13, s[4:7], s12 offen offset:2048 nt lds\n"
    "buffer_load_dwordx4 v4, s[4:7], s12 offen offset:3072 nt lds\n"
    "s_add_i32 s12, s16, 0x15000\n"
    "s_mov_b32 m0, s12\n"
    "s_nop 0\n"
    "buffer_load_dwordx4 v8, s[4:7], s20 offen nt lds\n"
    "buffer_load_dwordx4 v9, s[4:7], s20 offen offset:1024 nt lds\n"
    "buffer_load_dwordx4 v13, s[4:7], s20 offen offset:2048 nt lds\n"
    "buffer_load_dwordx4 v4, s[4:7], s20 offen offset:3072 nt lds\n"
    "s_add_i32 s12, s16, 0x16000\n"
    "s_mov_b32 m0, s12\n"
    "s_nop 0\n"
    "buffer_load_dwordx4 v8, s[4:7], s22 offen nt lds\n"
    "buffer_load_dwordx4 v9, s[4:7], s22 offen offset:1024 nt lds\n"
    "buffer_load_dwordx4 v13, s[4:7], s22 offen offset:2048 nt lds\n"
    "buffer_load_dwordx4 v4, s[4:7], s22 offen offset:3072 nt lds\n"
    "s_add_i32 s16, s16, 0x17000\n"
    "s_mov_b32 m0, s16\n"
    "s_nop 0\n"
    "buffer_load_dwordx4 v8, s[4:7], s23 offen nt lds\n"
    "buffer_load_dwordx4 v9, s[4:7], s23 offen offset:1024 nt lds\n"
    "buffer_load_dwordx4 v13, s[4:7], s23 offen offset:2048 nt lds\n"
    "buffer_load_dwordx4 v4, s[4:7], s23 offen offset:3072 nt lds\n"
    "v_and_b32_e32 v1, 63, v0\n"
    "v_and_b32_e32 v6, 7, v0\n"
    "s_movk_i32 s13, 0x1000\n"
    "s_movk_i32 s2, 0x2000\n"
    "v_bfe_u32 v7, v0, 3, 3\n"
    "s_add_i32 s12, s10, 0x17400\n"
    "s_branch .Lfb_role_a_tail\n"
    ".p2align 6\n"
    ".Lfb_role_b_tail:\n"
    "s_lshl_b32 s14, s12, 7\n"
    "v_bfe_u32 v108, v3, 4, 1\n"
    "v_lshl_or_b32 v109, v5, 3, s14\n"
    "v_lshlrev_b32_e32 v110, 1, v108\n"
    "v_and_b32_e32 v111, 4, v1\n"
    "v_or3_b32 v5, v111, v110, v109\n"
    "v_mov_b32_e32 v24, 0x800\n"
    "v_mov_b32_e32 v26, 0\n"
    "v_mov_b32_e32 v27, 0\n"
    "v_mov_b32_e32 v28, 0\n"
    "v_mov_b32_e32 v29, 0\n"
    "v_mov_b32_e32 v30, 0\n"
    "v_mov_b32_e32 v31, 0\n"
    "v_mov_b32_e32 v32, 0\n"
    "v_mov_b32_e32 v33, 0\n"
    "v_lshl_add_u32 v5, v5, 2, v24\n"
    "s_waitcnt vmcnt(4)\n"
    "s_mov_b32 s15, 0x5040100\n"
    "v_lshlrev_b32_e32 v34, 16, v22\n"
    "v_and_b32_e32 v22, 0xffff0000, v22\n"
    "v_mul_f32_e32 v24, 0xbfb8aa3b, v34\n"
    "v_mul_f32_e32 v25, 0xbfb8aa3b, v22\n"
    "v_exp_f32_e32 v24, v24\n"
    "v_exp_f32_e32 v25, v25\n"
    "v_and_b32_e32 v40, 0xffff0000, v23\n"
    "s_mov_b32 s16, 0x7060302\n"
    "s_movk_i32 s20, 0x800\n"
    "v_pk_add_f32 v[24:25], v[24:25], 1.0 op_sel_hi:[1,0]\n"
    "s_cmp_gt_i32 s2, 14\n"
    "v_div_scale_f32 v35, s[4:5], v25, v25, v22\n"
    "v_rcp_f32_e32 v36, v35\n"
    "s_nop 0\n"
    "v_fma_f32 v37, -v35, v36, 1.0\n"
    "v_fmac_f32_e32 v36, v37, v36\n"
    "v_div_scale_f32 v37, vcc, v22, v25, v22\n"
    "v_mul_f32_e32 v38, v37, v36\n"
    "v_fma_f32 v39, -v35, v38, v37\n"
    "v_fmac_f32_e32 v38, v39, v36\n"
    "v_fma_f32 v35, -v35, v38, v37\n"
    "v_div_scale_f32 v37, s[4:5], v24, v24, v34\n"
    "v_rcp_f32_e32 v39, v37\n"
    "v_div_fmas_f32 v35, v35, v36, v38\n"
    "v_div_fixup_f32 v25, v35, v25, v22\n"
    "v_lshlrev_b32_e32 v38, 16, v23\n"
    "v_fma_f32 v22, -v37, v39, 1.0\n"
    "v_fmac_f32_e32 v39, v22, v39\n"
    "v_mul_f32_e32 v22, 0xbfb8aa3b, v38\n"
    "v_mul_f32_e32 v23, 0xbfb8aa3b, v40\n"
    "v_exp_f32_e32 v22, v22\n"
    "v_exp_f32_e32 v23, v23\n"
    "v_div_scale_f32 v35, vcc, v34, v24, v34\n"
    "v_mul_f32_e32 v36, v35, v39\n"
    "v_fma_f32 v41, -v37, v36, v35\n"
    "v_fmac_f32_e32 v36, v41, v39\n"
    "v_pk_add_f32 v[22:23], v[22:23], 1.0 op_sel_hi:[1,0]\n"
    "v_fma_f32 v35, -v37, v36, v35\n"
    "v_div_scale_f32 v37, s[4:5], v23, v23, v40\n"
    "v_rcp_f32_e32 v41, v37\n"
    "v_div_fmas_f32 v35, v35, v39, v36\n"
    "v_div_fixup_f32 v24, v35, v24, v34\n"
    "v_cvt_pk_bf16_f32 v34, v24, v25\n"
    "v_fma_f32 v24, -v37, v41, 1.0\n"
    "v_fmac_f32_e32 v41, v24, v41\n"
    "v_div_scale_f32 v24, vcc, v40, v23, v40\n"
    "v_mul_f32_e32 v25, v24, v41\n"
    "v_fma_f32 v35, -v37, v25, v24\n"
    "v_fmac_f32_e32 v25, v35, v41\n"
    "v_div_scale_f32 v35, s[4:5], v22, v22, v38\n"
    "v_rcp_f32_e32 v36, v35\n"
    "v_fma_f32 v24, -v37, v25, v24\n"
    "v_div_fmas_f32 v24, v24, v41, v25\n"
    "v_div_fixup_f32 v23, v24, v23, v40\n"
    "v_fma_f32 v24, -v35, v36, 1.0\n"
    "v_fmac_f32_e32 v36, v24, v36\n"
    "v_div_scale_f32 v24, vcc, v38, v22, v38\n"
    "v_mul_f32_e32 v25, v24, v36\n"
    "v_fma_f32 v37, -v35, v25, v24\n"
    "v_fmac_f32_e32 v25, v37, v36\n"
    "v_fma_f32 v24, -v35, v25, v24\n"
    "v_div_fmas_f32 v24, v24, v36, v25\n"
    "v_div_fixup_f32 v22, v24, v22, v38\n"
    "v_cvt_pk_bf16_f32 v35, v22, v23\n"
    "s_waitcnt vmcnt(0)\n"
    "s_nop 0\n"
    "v_perm_b32 v22, v10, v6, s15\n"
    "v_perm_b32 v23, v11, v7, s15\n"
    "v_perm_b32 v24, v12, v8, s15\n"
    "v_perm_b32 v25, v13, v9, s15\n"
    "v_perm_b32 v6, v10, v6, s16\n"
    "v_perm_b32 v7, v11, v7, s16\n"
    "v_perm_b32 v8, v12, v8, s16\n"
    "v_perm_b32 v9, v13, v9, s16\n"
    "v_perm_b32 v10, v18, v14, s15\n"
    "v_perm_b32 v11, v19, v15, s15\n"
    "v_perm_b32 v12, v20, v16, s15\n"
    "v_perm_b32 v13, v21, v17, s15\n"
    "v_perm_b32 v14, v18, v14, s16\n"
    "v_perm_b32 v15, v19, v15, s16\n"
    "v_perm_b32 v16, v20, v16, s16\n"
    "v_perm_b32 v17, v21, v17, s16\n"
    "v_dot2c_f32_bf16_e32 v26, v22, v34\n"
    "v_dot2c_f32_bf16_e32 v27, v6, v34\n"
    "v_dot2c_f32_bf16_e32 v28, v23, v34\n"
    "v_dot2c_f32_bf16_e32 v29, v7, v34\n"
    "v_dot2c_f32_bf16_e32 v30, v24, v34\n"
    "v_dot2c_f32_bf16_e32 v31, v8, v34\n"
    "v_dot2c_f32_bf16_e32 v32, v25, v34\n"
    "v_dot2c_f32_bf16_e32 v33, v9, v34\n"
    "s_nop 0\n"
    "v_dot2c_f32_bf16_e32 v26, v10, v35\n"
    "v_dot2c_f32_bf16_e32 v27, v14, v35\n"
    "v_dot2c_f32_bf16_e32 v28, v11, v35\n"
    "v_dot2c_f32_bf16_e32 v29, v15, v35\n"
    "v_dot2c_f32_bf16_e32 v30, v12, v35\n"
    "v_dot2c_f32_bf16_e32 v31, v16, v35\n"
    "v_dot2c_f32_bf16_e32 v32, v13, v35\n"
    "v_dot2c_f32_bf16_e32 v33, v17, v35\n"
    "v_permlane32_swap_b32_e32 v26, v30\n"
    "v_permlane32_swap_b32_e32 v27, v31\n"
    "v_permlane32_swap_b32_e32 v28, v32\n"
    "v_permlane32_swap_b32_e32 v29, v33\n"
    "v_add_f32_e32 v6, v26, v30\n"
    "v_add_f32_e32 v7, v27, v31\n"
    "v_add_f32_e32 v8, v28, v32\n"
    "v_add_f32_e32 v9, v29, v33\n"
    "s_nop 0\n"
    "v_permlane16_swap_b32_e32 v6, v8\n"
    "v_permlane16_swap_b32_e32 v7, v9\n"
    "v_pk_add_f32 v[6:7], v[6:7], v[8:9]\n"
    "ds_write_b64 v5, v[6:7]\n"
    "s_waitcnt lgkmcnt(0)\n"
    "s_barrier\n"
    "s_waitcnt lgkmcnt(0)\n"
    "s_barrier\n"
    "s_cbranch_scc1 .Lfb_role_b_3\n"
    "s_load_dwordx4 s[24:27], s[0:1], 0x28\n"
    "s_load_dwordx2 s[2:3], s[0:1], 0x0\n"
    "v_and_b32_e32 v136, 7, v0\n"
    "s_load_dwordx2 s[4:5], s[0:1], 0x58\n"
    "s_load_dwordx2 s[8:9], s[0:1], 0x38\n"
    "v_cmp_gt_u32_e32 vcc, 4, v136\n"
    "s_waitcnt lgkmcnt(0)\n"
    "s_mul_i32 s0, s10, 0xe0\n"
    "v_mov_b32_e32 v5, s27\n"
    "v_mov_b32_e32 v6, s25\n"
    "s_lshl_b32 s1, s17, 5\n"
    "v_cndmask_b32_e32 v7, v5, v6, vcc\n"
    "v_mov_b32_e32 v5, s26\n"
    "v_mov_b32_e32 v6, s24\n"
    "s_add_i32 s0, s1, s0\n"
    "v_cndmask_b32_e32 v6, v5, v6, vcc\n"
    "v_lshlrev_b32_e32 v5, 5, v1\n"
    "s_movk_i32 s1, 0x62\n"
    "v_bitop3_b32 v5, v2, s1, v5 bitop3:0xc8\n"
    "v_or_b32_e32 v5, s14, v5\n"
    "s_movk_i32 s1, 0x700\n"
    "v_mul_lo_u32 v102, v5, s1\n"
    "v_lshlrev_b32_e32 v5, 3, v0\n"
    "v_lshl_add_u64 v[6:7], v[102:103], 1, v[6:7]\n"
    "s_ashr_i32 s1, s0, 31\n"
    "v_and_b32_e32 v137, 24, v5\n"
    "s_mov_b32 s13, 0x7f800000\n"
    "v_lshl_add_u64 v[6:7], s[0:1], 1, v[6:7]\n"
    "v_lshlrev_b32_e32 v102, 1, v137\n"
    "v_lshl_add_u64 v[30:31], v[6:7], 0, v[102:103]\n"
    "global_load_dwordx4 v[6:9], v[30:31], off nt\n"
    "s_mul_i32 s21, s10, 7\n"
    "global_load_dwordx4 v[10:13], v[30:31], off offset:3584 nt\n"
    "s_mov_b64 s[10:11], 0x3800\n"
    "v_lshl_add_u64 v[18:19], v[30:31], 0, s[10:11]\n"
    "global_load_dwordx4 v[14:17], v[18:19], off nt\n"
    "global_load_dwordx4 v[18:21], v[18:19], off offset:3584 nt\n"
    "s_mov_b64 s[10:11], 0x7000\n"
    "v_lshl_add_u64 v[26:27], v[30:31], 0, s[10:11]\n"
    "global_load_dwordx4 v[22:25], v[26:27], off nt\n"
    "global_load_dwordx4 v[26:29], v[26:27], off offset:3584 nt\n"
    "s_mov_b64 s[10:11], 0xa800\n"
    "v_lshl_add_u64 v[32:33], v[30:31], 0, s[10:11]\n"
    "global_load_dwordx4 v[62:65], v[32:33], off nt\n"
    "global_load_dwordx4 v[66:69], v[32:33], off offset:3584 nt\n"
    "s_mov_b64 s[10:11], 0xe000\n"
    "v_lshl_add_u64 v[32:33], v[30:31], 0, s[10:11]\n"
    "global_load_dwordx4 v[70:73], v[32:33], off nt\n"
    "global_load_dwordx4 v[74:77], v[32:33], off offset:3584 nt\n"
    "s_mov_b64 s[10:11], 0x11800\n"
    "v_lshl_add_u64 v[32:33], v[30:31], 0, s[10:11]\n"
    "global_load_dwordx4 v[78:81], v[32:33], off nt\n"
    "global_load_dwordx4 v[82:85], v[32:33], off offset:3584 nt\n"
    "s_mov_b64 s[10:11], 0x15000\n"
    "v_lshl_add_u64 v[32:33], v[30:31], 0, s[10:11]\n"
    "global_load_dwordx4 v[86:89], v[32:33], off nt\n"
    "global_load_dwordx4 v[90:93], v[32:33], off offset:3584 nt\n"
    "s_mov_b64 s[10:11], 0x18800\n"
    "v_lshlrev_b32_e32 v5, 2, v0\n"
    "v_lshrrev_b32_e32 v138, 5, v3\n"
    "v_lshl_add_u64 v[30:31], v[30:31], 0, s[10:11]\n"
    "global_load_dwordx4 v[94:97], v[30:31], off nt\n"
    "v_and_b32_e32 v102, 0x7c, v5\n"
    "v_lshlrev_b32_e32 v3, 1, v138\n"
    "v_lshlrev_b32_e32 v141, 3, v2\n"
    "global_load_dwordx4 v[104:107], v[30:31], off offset:3584 nt\n"
    "v_or3_b32 v139, v3, v102, s14\n"
    "v_or_b32_e32 v2, s0, v141\n"
    "s_lshl_b32 s0, s12, 8\n"
    "v_lshlrev_b32_e32 v3, 1, v139\n"
    "global_load_dword v140, v3, s[2:3]\n"
    "v_lshl_add_u32 v2, v2, 10, s0\n"
    "s_and_b32 s9, s9, 0xffff\n"
    "s_mov_b32 s10, 0x1c0000\n"
    "s_mov_b32 s11, s7\n"
    "v_or_b32_e32 v2, v2, v4\n"
    "buffer_load_dwordx4 v[112:115], v2, s[8:11], s19 offen nt\n"
    "s_movk_i32 s1, 0x400\n"
    "buffer_load_dwordx4 v[116:119], v2, s[8:11], s1 offen nt\n"
    "buffer_load_dwordx4 v[120:123], v2, s[8:11], s20 offen nt\n"
    "s_movk_i32 s1, 0xc00\n"
    "buffer_load_dwordx4 v[124:127], v2, s[8:11], s1 offen nt\n"
    "s_movk_i32 s1, 0x1000\n"
    "buffer_load_dwordx4 v[128:131], v2, s[8:11], s1 offen nt\n"
    "s_movk_i32 s2, 0x1400\n"
    "buffer_load_dwordx4 v[132:135], v2, s[8:11], s2 offen nt\n"
    "buffer_load_dwordx4 v[30:33], v2, s[8:11], s18 offen nt\n"
    "s_movk_i32 s2, 0x1c00\n"
    "buffer_load_dwordx4 v[98:101], v2, s[8:11], s2 offen nt\n"
    "s_waitcnt vmcnt(8)\n"
    "s_mov_b32 s6, 0x83400\n"
    "v_perm_b32 v34, v10, v6, s15\n"
    "v_perm_b32 v35, v11, v7, s15\n"
    "v_perm_b32 v36, v12, v8, s15\n"
    "v_perm_b32 v37, v13, v9, s15\n"
    "v_perm_b32 v42, v10, v6, s16\n"
    "v_perm_b32 v43, v11, v7, s16\n"
    "v_perm_b32 v44, v12, v8, s16\n"
    "v_perm_b32 v45, v13, v9, s16\n"
    "v_perm_b32 v38, v18, v14, s15\n"
    "v_perm_b32 v39, v19, v15, s15\n"
    "v_perm_b32 v40, v20, v16, s15\n"
    "v_perm_b32 v41, v21, v17, s15\n"
    "v_perm_b32 v50, v18, v14, s16\n"
    "v_perm_b32 v51, v19, v15, s16\n"
    "v_perm_b32 v52, v20, v16, s16\n"
    "v_perm_b32 v53, v21, v17, s16\n"
    "v_perm_b32 v46, v26, v22, s15\n"
    "v_perm_b32 v47, v27, v23, s15\n"
    "v_perm_b32 v48, v28, v24, s15\n"
    "v_perm_b32 v49, v29, v25, s15\n"
    "v_perm_b32 v58, v26, v22, s16\n"
    "v_perm_b32 v59, v27, v23, s16\n"
    "v_perm_b32 v60, v28, v24, s16\n"
    "v_perm_b32 v61, v29, v25, s16\n"
    "v_perm_b32 v54, v66, v62, s15\n"
    "v_perm_b32 v55, v67, v63, s15\n"
    "v_perm_b32 v56, v68, v64, s15\n"
    "v_perm_b32 v57, v69, v65, s15\n"
    "v_perm_b32 v66, v66, v62, s16\n"
    "v_perm_b32 v67, v67, v63, s16\n"
    "v_perm_b32 v68, v68, v64, s16\n"
    "v_perm_b32 v69, v69, v65, s16\n"
    "v_perm_b32 v62, v74, v70, s15\n"
    "v_perm_b32 v63, v75, v71, s15\n"
    "v_perm_b32 v64, v76, v72, s15\n"
    "v_perm_b32 v65, v77, v73, s15\n"
    "v_perm_b32 v74, v74, v70, s16\n"
    "v_perm_b32 v75, v75, v71, s16\n"
    "v_perm_b32 v76, v76, v72, s16\n"
    "v_perm_b32 v77, v77, v73, s16\n"
    "v_perm_b32 v70, v82, v78, s15\n"
    "v_perm_b32 v71, v83, v79, s15\n"
    "v_perm_b32 v72, v84, v80, s15\n"
    "v_perm_b32 v73, v85, v81, s15\n"
    "v_perm_b32 v82, v82, v78, s16\n"
    "v_perm_b32 v83, v83, v79, s16\n"
    "v_perm_b32 v84, v84, v80, s16\n"
    "v_perm_b32 v85, v85, v81, s16\n"
    "v_perm_b32 v78, v90, v86, s15\n"
    "v_perm_b32 v79, v91, v87, s15\n"
    "v_perm_b32 v80, v92, v88, s15\n"
    "v_perm_b32 v81, v93, v89, s15\n"
    "v_perm_b32 v90, v90, v86, s16\n"
    "v_perm_b32 v91, v91, v87, s16\n"
    "v_perm_b32 v92, v92, v88, s16\n"
    "v_perm_b32 v93, v93, v89, s16\n"
    "v_perm_b32 v86, v104, v94, s15\n"
    "v_perm_b32 v87, v105, v95, s15\n"
    "v_perm_b32 v88, v106, v96, s15\n"
    "v_perm_b32 v89, v107, v97, s15\n"
    "v_perm_b32 v94, v104, v94, s16\n"
    "v_perm_b32 v95, v105, v95, s16\n"
    "v_perm_b32 v96, v106, v96, s16\n"
    "v_perm_b32 v97, v107, v97, s16\n"
    "s_waitcnt vmcnt(0)\n"
    "s_and_b32 s5, s5, 0xffff\n"
    "v_perm_b32 v2, v116, v112, s15\n"
    "v_perm_b32 v3, v117, v113, s15\n"
    "v_perm_b32 v4, v118, v114, s15\n"
    "v_perm_b32 v5, v119, v115, s15\n"
    "v_perm_b32 v10, v116, v112, s16\n"
    "v_perm_b32 v11, v117, v113, s16\n"
    "v_perm_b32 v12, v118, v114, s16\n"
    "v_perm_b32 v13, v119, v115, s16\n"
    "v_perm_b32 v6, v124, v120, s15\n"
    "v_perm_b32 v7, v125, v121, s15\n"
    "v_perm_b32 v8, v126, v122, s15\n"
    "v_perm_b32 v9, v127, v123, s15\n"
    "v_perm_b32 v18, v124, v120, s16\n"
    "v_perm_b32 v19, v125, v121, s16\n"
    "v_perm_b32 v20, v126, v122, s16\n"
    "v_perm_b32 v21, v127, v123, s16\n"
    "v_perm_b32 v14, v132, v128, s15\n"
    "v_perm_b32 v15, v133, v129, s15\n"
    "v_perm_b32 v16, v134, v130, s15\n"
    "v_perm_b32 v17, v135, v131, s15\n"
    "v_perm_b32 v26, v132, v128, s16\n"
    "v_perm_b32 v27, v133, v129, s16\n"
    "v_perm_b32 v28, v134, v130, s16\n"
    "v_perm_b32 v29, v135, v131, s16\n"
    "v_perm_b32 v22, v98, v30, s15\n"
    "v_perm_b32 v23, v99, v31, s15\n"
    "v_perm_b32 v24, v100, v32, s15\n"
    "v_perm_b32 v25, v101, v33, s15\n"
    "v_perm_b32 v30, v98, v30, s16\n"
    "v_perm_b32 v31, v99, v31, s16\n"
    "v_perm_b32 v32, v100, v32, s16\n"
    "v_perm_b32 v33, v101, v33, s16\n"
    "s_waitcnt lgkmcnt(0)\n"
    "s_barrier\n"
    "v_lshlrev_b32_e32 v98, 2, v139\n"
    "v_add_u32_e32 v99, 0x7f400, v98\n"
    "v_add_u32_e32 v98, 0x81400, v98\n"
    "s_mov_b32 s2, 0x20000\n"
    "1:\n"
    "buffer_load_dwordx2 v[224:225], v99, s[4:7], 0 offen offset:0 sc1\n"
    "buffer_load_dwordx2 v[226:227], v98, s[4:7], 0 offen offset:0 sc1\n"
    "buffer_load_dwordx2 v[228:229], v99, s[4:7], 0 offen offset:2048 sc1\n"
    "buffer_load_dwordx2 v[230:231], v98, s[4:7], 0 offen offset:2048 sc1\n"
    "buffer_load_dwordx2 v[232:233], v99, s[4:7], s1 offen offset:0 sc1\n"
    "buffer_load_dwordx2 v[234:235], v98, s[4:7], s1 offen offset:0 sc1\n"
    "s_waitcnt vmcnt(0)\n"
    "v_max3_u32 v100, v224, v225, v226\n"
    "v_max3_u32 v100, v100, v227, v228\n"
    "v_max3_u32 v100, v100, v229, v230\n"
    "v_max3_u32 v100, v100, v231, v232\n"
    "v_max3_u32 v100, v100, v233, v234\n"
    "v_max3_u32 v100, v100, v235, v235\n"
    "v_cmp_eq_u32 vcc, -1, v100\n"
    "s_cbranch_vccz 2f\n"
    "s_mov_b64 exec, vcc\n"
    "s_sub_u32 s2, s2, 1\n"
    "s_cbranch_scc0 1b\n"
    "2:\n"
    "s_mov_b64 exec, -1\n"
    "v_or_b32_e32 v1, v137, v1\n"
    "s_add_i32 s17, s17, s21\n"
    "v_pk_add_f32 v[100:101], v[232:233], v[234:235]\n"
    "v_lshl_add_u32 v112, v138, 9, s14\n"
    "v_lshlrev_b32_e32 v1, 3, v1\n"
    "v_and_b32_e32 v0, 4, v0\n"
    "v_pk_add_f32 v[98:99], v[224:225], v[226:227]\n"
    "v_cvt_pk_bf16_f32 v100, 0, v100\n"
    "v_cvt_pk_bf16_f32 v101, 0, v101\n"
    "v_or_b32_e32 v102, v112, v102\n"
    "v_mov_b32_e32 v112, 0x1bc00\n"
    "s_lshl_b32 s1, s17, 11\n"
    "v_or3_b32 v0, v1, v0, s0\n"
    "s_lshl_b32 s0, s12, 3\n"
    "v_cvt_pk_bf16_f32 v106, 0, v98\n"
    "v_cvt_pk_bf16_f32 v107, 0, v99\n"
    "v_pk_add_f32 v[98:99], v[228:229], v[230:231]\n"
    "v_add_f32_e32 v100, 1.0, v100\n"
    "v_add_f32_e32 v101, 1.0, v101\n"
    "v_lshl_add_u32 v128, v102, 2, v112\n"
    "v_or3_b32 v102, v111, v109, v110\n"
    "s_add_i32 s1, s1, 0x63400\n"
    "v_add_u32_e32 v118, 0x840, v0\n"
    "v_or_b32_e32 v0, v141, v136\n"
    "v_mov_b32_e32 v1, 0x840\n"
    "s_addk_i32 s0, 0x800\n"
    "v_lshlrev_b32_e32 v104, 16, v140\n"
    "v_and_b32_e32 v105, 0xffff0000, v140\n"
    "v_cvt_pk_bf16_f32 v98, 0, v98\n"
    "v_cvt_pk_bf16_f32 v99, 0, v99\n"
    "v_cvt_pk_bf16_f32 v100, 0, v100\n"
    "v_cvt_pk_bf16_f32 v101, 0, v101\n"
    "v_lshl_add_u32 v102, v102, 2, s1\n"
    "v_lshl_add_u32 v117, v0, 3, v1\n"
    "v_lshl_or_b32 v127, v108, 2, s0\n"
    "v_mov_b32_e32 v122, 0\n"
    "v_mov_b32_e32 v121, 0\n"
    "v_mov_b32_e32 v120, 0\n"
    "v_mov_b32_e32 v119, 0\n"
    "v_mov_b32_e32 v126, 0\n"
    "v_mov_b32_e32 v125, 0\n"
    "v_mov_b32_e32 v124, 0\n"
    "v_mov_b32_e32 v123, 0\n"
    "v_mov_b32_e32 v113, 0\n"
    "v_mov_b32_e32 v112, 0\n"
    "v_mov_b32_e32 v111, 0\n"
    "v_mov_b32_e32 v110, 0\n"
    "v_mov_b32_e32 v116, 0\n"
    "v_mov_b32_e32 v115, 0\n"
    "v_mov_b32_e32 v114, 0\n"
    "s_mov_b32 s2, 0x9800\n"
    "s_mov_b32 s3, 0x13000\n"
    "s_mov_b32 s8, 0x1c800\n"
    "s_mov_b32 s9, 0x26000\n"
    "s_mov_b32 s10, 0x2f800\n"
    "s_mov_b32 s11, 0x39000\n"
    "s_mov_b32 s12, 0x42800\n"
    "s_waitcnt lgkmcnt(0)\n"
    "s_barrier\n"
    "buffer_load_dwordx4 v[224:227], v128, s[4:7], 0 offen sc1\n"
    "buffer_load_dwordx4 v[228:231], v128, s[4:7], s2 offen sc1\n"
    "buffer_load_dwordx4 v[232:235], v128, s[4:7], s3 offen sc1\n"
    "buffer_load_dwordx4 v[236:239], v128, s[4:7], s8 offen sc1\n"
    "buffer_load_dwordx4 v[240:243], v128, s[4:7], s9 offen sc1\n"
    "buffer_load_dwordx4 v[244:247], v128, s[4:7], s10 offen sc1\n"
    "buffer_load_dwordx4 v[248:251], v128, s[4:7], s11 offen sc1\n"
    "buffer_load_dwordx4 v[252:255], v128, s[4:7], s12 offen sc1\n"
    "s_waitcnt vmcnt(0)\n"
    "s_nop 0\n"
    "v_pk_add_f32 v[0:1], v[226:227], v[230:231]\n"
    "v_pk_add_f32 v[108:109], v[224:225], v[228:229]\n"
    "v_pk_add_f32 v[130:131], v[234:235], v[238:239]\n"
    "v_pk_add_f32 v[132:133], v[232:233], v[236:237]\n"
    "v_pk_add_f32 v[0:1], v[0:1], v[130:131]\n"
    "v_pk_add_f32 v[108:109], v[108:109], v[132:133]\n"
    "v_pk_add_f32 v[130:131], v[242:243], v[246:247]\n"
    "v_pk_add_f32 v[132:133], v[240:241], v[244:245]\n"
    "v_pk_add_f32 v[134:135], v[250:251], v[254:255]\n"
    "v_pk_add_f32 v[136:137], v[248:249], v[252:253]\n"
    "v_pk_add_f32 v[130:131], v[130:131], v[134:135]\n"
    "v_pk_add_f32 v[132:133], v[132:133], v[136:137]\n"
    "v_pk_add_f32 v[0:1], v[0:1], v[130:131]\n"
    "v_pk_add_f32 v[108:109], v[108:109], v[132:133]\n"
    "v_cmp_u_f32_e64 s[0:1], v0, v1\n"
    "v_cmp_u_f32_e32 vcc, v108, v109\n"
    "s_or_b64 vcc, vcc, s[0:1]\n"
    "s_cbranch_vccnz .Lfb_role_b_4\n"
    ".Lfb_role_b_2:\n"
    "s_nop 0\n"
    "v_permlane32_swap_b32_e32 v108, v0\n"
    "v_add_f32_e32 v0, v108, v0\n"
    "v_cvt_pk_bf16_f32 v0, 0, v0\n"
    "v_mul_f32_e32 v0, v106, v0\n"
    "v_cvt_pk_bf16_f32 v0, 0, v0\n"
    "v_add_f32_e32 v0, v104, v0\n"
    "v_permlane32_swap_b32_e32 v109, v1\n"
    "v_cvt_pk_bf16_f32 v132, 0, v0\n"
    "v_add_f32_e32 v0, v109, v1\n"
    "v_cvt_pk_bf16_f32 v0, 0, v0\n"
    "v_mul_f32_e32 v0, v107, v0\n"
    "v_cvt_pk_bf16_f32 v0, 0, v0\n"
    "v_add_f32_e32 v0, v105, v0\n"
    "v_cvt_pk_bf16_f32 v109, 0, v0\n"
    "v_mul_f32_e32 v1, v109, v109\n"
    "v_add_f32_e32 v0, v132, v109\n"
    "v_fmac_f32_e32 v1, v132, v132\n"
    "v_mov_b32_e32 v108, 0\n"
    "v_add_f32_dpp v0, v0, v0 quad_perm:[1,0,3,2] row_mask:0xf bank_mask:0xf bound_ctrl:1\n"
    "v_add_f32_dpp v1, v1, v1 quad_perm:[1,0,3,2] row_mask:0xf bank_mask:0xf bound_ctrl:1\n"
    "s_mov_b32 s0, 0x3b000000\n"
    "v_add_f32_dpp v0, v0, v0 quad_perm:[2,3,0,1] row_mask:0xf bank_mask:0xf bound_ctrl:1\n"
    "v_add_f32_dpp v1, v1, v1 quad_perm:[2,3,0,1] row_mask:0xf bank_mask:0xf bound_ctrl:1\n"
    "s_nop 0\n"
    "v_add_f32_dpp v0, v0, v0 row_half_mirror row_mask:0xf bank_mask:0xf bound_ctrl:1\n"
    "v_add_f32_dpp v1, v1, v1 row_half_mirror row_mask:0xf bank_mask:0xf bound_ctrl:1\n"
    "s_nop 0\n"
    "v_add_f32_dpp v0, v0, v0 row_ror:8 row_mask:0xf bank_mask:0xf bound_ctrl:1\n"
    "v_add_f32_dpp v1, v1, v1 row_ror:8 row_mask:0xf bank_mask:0xf bound_ctrl:1\n"
    "s_nop 1\n"
    "v_permlane16_swap_b32_e32 v0, v1\n"
    "v_add_f32_e32 v0, v0, v1\n"
    "v_mov_b32_e32 v1, v0\n"
    "s_nop 1\n"
    "v_permlane32_swap_b32_e32 v0, v1\n"
    "v_add_f32_e32 v0, v0, v1\n"
    "ds_write_b32 v127, v0\n"
    "s_waitcnt lgkmcnt(0)\n"
    "s_barrier\n"
    "ds_read_b128 v[104:107], v108 offset:2048\n"
    "ds_read_b128 v[128:131], v108 offset:2064\n"
    "s_waitcnt lgkmcnt(0)\n"
    "v_pk_add_f32 v[0:1], v[106:107], v[130:131]\n"
    "v_pk_add_f32 v[104:105], v[104:105], v[128:129]\n"
    "s_nop 0\n"
    "v_pk_add_f32 v[0:1], v[104:105], v[0:1]\n"
    "s_nop 0\n"
    "v_pk_mul_f32 v[0:1], v[0:1], s[0:1] op_sel_hi:[1,0]\n"
    "s_nop 0\n"
    "v_fma_f32 v1, -v0, v0, v1\n"
    "v_med3_f32 v1, v1, s13, 0\n"
    "v_cvt_pk_bf16_f32 v1, 0, v1\n"
    "v_add_f32_e32 v1, 0x3727c5ac, v1\n"
    "v_cvt_pk_bf16_f32 v1, 0, v1\n"
    "v_sqrt_f32_e32 v1, v1\n"
    "v_cvt_pk_bf16_f32 v104, 0, v0\n"
    "v_sub_f32_e32 v0, v132, v104\n"
    "v_cvt_pk_bf16_f32 v0, 0, v0\n"
    "v_cvt_pk_bf16_f32 v1, 0, v1\n"
    "v_rcp_f32_e32 v1, v1\n"
    "s_nop 0\n"
    "v_mul_f32_e32 v0, v1, v0\n"
    "v_cvt_pk_bf16_f32 v0, 0, v0\n"
    "v_mul_f32_e32 v0, v100, v0\n"
    "v_sub_f32_e32 v100, v109, v104\n"
    "v_cvt_pk_bf16_f32 v100, 0, v100\n"
    "v_mul_f32_e32 v1, v1, v100\n"
    "v_cvt_pk_bf16_f32 v1, 0, v1\n"
    "v_mul_f32_e32 v1, v101, v1\n"
    "v_cvt_pk_bf16_f32 v0, 0, v0\n"
    "v_cvt_pk_bf16_f32 v1, 0, v1\n"
    "v_pk_add_f32 v[0:1], v[98:99], v[0:1]\n"
    "s_nop 0\n"
    "v_cvt_pk_bf16_f32 v0, v0, v1\n"
    "v_mov_b32_e32 v1, v0\n"
    "s_nop 0\n"
    "v_mov_b32_dpp v0, v0 row_shl:4 row_mask:0xf bank_mask:0x5\n"
    "v_mov_b32_dpp v1, v1 row_shr:4 row_mask:0xf bank_mask:0xa\n"
    "s_nop 0\n"
    "v_mov_b32_dpp v101, v0 quad_perm:[0,0,0,0] row_mask:0xf bank_mask:0xf bound_ctrl:1\n"
    "v_mov_b32_dpp v98, v1 quad_perm:[0,0,0,0] row_mask:0xf bank_mask:0xf bound_ctrl:1\n"
    "v_mov_b32_dpp v99, v1 quad_perm:[1,1,1,1] row_mask:0xf bank_mask:0xf bound_ctrl:1\n"
    "v_dot2c_f32_bf16_e32 v122, v34, v98\n"
    "v_dot2c_f32_bf16_e32 v121, v42, v98\n"
    "v_dot2c_f32_bf16_e32 v120, v35, v98\n"
    "v_dot2c_f32_bf16_e32 v119, v43, v98\n"
    "v_dot2c_f32_bf16_e32 v126, v36, v98\n"
    "v_dot2c_f32_bf16_e32 v125, v44, v98\n"
    "v_dot2c_f32_bf16_e32 v124, v37, v98\n"
    "v_dot2c_f32_bf16_e32 v123, v45, v98\n"
    "v_mov_b32_dpp v100, v1 quad_perm:[2,2,2,2] row_mask:0xf bank_mask:0xf bound_ctrl:1\n"
    "v_dot2c_f32_bf16_e32 v122, v38, v99\n"
    "v_dot2c_f32_bf16_e32 v121, v50, v99\n"
    "v_dot2c_f32_bf16_e32 v120, v39, v99\n"
    "v_dot2c_f32_bf16_e32 v119, v51, v99\n"
    "v_dot2c_f32_bf16_e32 v126, v40, v99\n"
    "v_dot2c_f32_bf16_e32 v125, v52, v99\n"
    "v_dot2c_f32_bf16_e32 v124, v41, v99\n"
    "v_dot2c_f32_bf16_e32 v123, v53, v99\n"
    "v_mov_b32_dpp v1, v1 quad_perm:[3,3,3,3] row_mask:0xf bank_mask:0xf bound_ctrl:1\n"
    "v_dot2c_f32_bf16_e32 v122, v46, v100\n"
    "v_dot2c_f32_bf16_e32 v121, v58, v100\n"
    "v_dot2c_f32_bf16_e32 v120, v47, v100\n"
    "v_dot2c_f32_bf16_e32 v119, v59, v100\n"
    "v_dot2c_f32_bf16_e32 v126, v48, v100\n"
    "v_dot2c_f32_bf16_e32 v125, v60, v100\n"
    "v_dot2c_f32_bf16_e32 v124, v49, v100\n"
    "v_dot2c_f32_bf16_e32 v123, v61, v100\n"
    "v_dot2c_f32_bf16_e32 v122, v54, v1\n"
    "v_dot2c_f32_bf16_e32 v121, v66, v1\n"
    "v_dot2c_f32_bf16_e32 v120, v55, v1\n"
    "v_dot2c_f32_bf16_e32 v119, v67, v1\n"
    "v_dot2c_f32_bf16_e32 v126, v56, v1\n"
    "v_dot2c_f32_bf16_e32 v125, v68, v1\n"
    "v_dot2c_f32_bf16_e32 v124, v57, v1\n"
    "v_dot2c_f32_bf16_e32 v123, v69, v1\n"
    "v_mov_b32_dpp v104, v0 quad_perm:[1,1,1,1] row_mask:0xf bank_mask:0xf bound_ctrl:1\n"
    "v_dot2c_f32_bf16_e32 v122, v62, v101\n"
    "v_dot2c_f32_bf16_e32 v121, v74, v101\n"
    "v_dot2c_f32_bf16_e32 v120, v63, v101\n"
    "v_dot2c_f32_bf16_e32 v119, v75, v101\n"
    "v_dot2c_f32_bf16_e32 v126, v64, v101\n"
    "v_dot2c_f32_bf16_e32 v125, v76, v101\n"
    "v_dot2c_f32_bf16_e32 v124, v65, v101\n"
    "v_dot2c_f32_bf16_e32 v123, v77, v101\n"
    "v_mov_b32_dpp v105, v0 quad_perm:[2,2,2,2] row_mask:0xf bank_mask:0xf bound_ctrl:1\n"
    "v_dot2c_f32_bf16_e32 v122, v70, v104\n"
    "v_dot2c_f32_bf16_e32 v121, v82, v104\n"
    "v_dot2c_f32_bf16_e32 v120, v71, v104\n"
    "v_dot2c_f32_bf16_e32 v119, v83, v104\n"
    "v_dot2c_f32_bf16_e32 v126, v72, v104\n"
    "v_dot2c_f32_bf16_e32 v125, v84, v104\n"
    "v_dot2c_f32_bf16_e32 v124, v73, v104\n"
    "v_dot2c_f32_bf16_e32 v123, v85, v104\n"
    "v_mov_b32_dpp v0, v0 quad_perm:[3,3,3,3] row_mask:0xf bank_mask:0xf bound_ctrl:1\n"
    "v_dot2c_f32_bf16_e32 v122, v78, v105\n"
    "v_dot2c_f32_bf16_e32 v121, v90, v105\n"
    "v_dot2c_f32_bf16_e32 v120, v79, v105\n"
    "v_dot2c_f32_bf16_e32 v119, v91, v105\n"
    "v_dot2c_f32_bf16_e32 v126, v80, v105\n"
    "v_dot2c_f32_bf16_e32 v125, v92, v105\n"
    "v_dot2c_f32_bf16_e32 v124, v81, v105\n"
    "v_dot2c_f32_bf16_e32 v123, v93, v105\n"
    "v_dot2c_f32_bf16_e32 v122, v86, v0\n"
    "v_dot2c_f32_bf16_e32 v121, v94, v0\n"
    "v_dot2c_f32_bf16_e32 v120, v87, v0\n"
    "v_dot2c_f32_bf16_e32 v119, v95, v0\n"
    "v_dot2c_f32_bf16_e32 v126, v88, v0\n"
    "v_dot2c_f32_bf16_e32 v125, v96, v0\n"
    "v_dot2c_f32_bf16_e32 v124, v89, v0\n"
    "v_dot2c_f32_bf16_e32 v123, v97, v0\n"
    "v_permlane32_swap_b32_e32 v122, v126\n"
    "v_permlane32_swap_b32_e32 v121, v125\n"
    "v_permlane32_swap_b32_e32 v120, v124\n"
    "v_permlane32_swap_b32_e32 v119, v123\n"
    "v_add_f32_e32 v0, v122, v126\n"
    "v_add_f32_e32 v1, v121, v125\n"
    "v_add_f32_e32 v34, v120, v124\n"
    "v_add_f32_e32 v35, v119, v123\n"
    "s_nop 0\n"
    "v_permlane16_swap_b32_e32 v0, v34\n"
    "v_permlane16_swap_b32_e32 v1, v35\n"
    "v_add_f32_e32 v0, v0, v34\n"
    "v_add_f32_e32 v1, v1, v35\n"
    "s_nop 0\n"
    "v_add_f32_dpp v0, v0, v0 row_ror:8 row_mask:0xf bank_mask:0xf bound_ctrl:1\n"
    "v_add_f32_dpp v1, v1, v1 row_ror:8 row_mask:0xf bank_mask:0xf bound_ctrl:1\n"
    "s_nop 1\n"
    "v_mov_b32_dpp v0, v1 quad_perm:[0,1,2,3] row_mask:0xf bank_mask:0xc\n"
    "ds_write_b32 v118, v0\n"
    "s_waitcnt lgkmcnt(0)\n"
    "s_barrier\n"
    "ds_read2_b64 v[34:37], v117 offset1:32\n"
    "ds_read2_b64 v[38:41], v117 offset0:64 offset1:96\n"
    "s_waitcnt lgkmcnt(1)\n"
    "v_pk_add_f32 v[0:1], v[34:35], v[36:37]\n"
    "s_waitcnt lgkmcnt(0)\n"
    "v_pk_add_f32 v[34:35], v[38:39], v[40:41]\n"
    "s_nop 0\n"
    "v_pk_add_f32 v[0:1], v[0:1], v[34:35]\n"
    "s_nop 0\n"
    "v_cvt_pk_bf16_f32 v0, 0, v0\n"
    "v_mul_f32_e32 v35, 0x3d372713, v0\n"
    "v_mul_f32_e32 v34, 0xc0135761, v0\n"
    "v_fma_f32 v35, v35, v0, 1.0\n"
    "v_mul_f32_e32 v34, v34, v35\n"
    "v_exp_f32_e32 v34, v34\n"
    "v_cvt_pk_bf16_f32 v1, 0, v1\n"
    "v_add_f32_e32 v34, 1.0, v34\n"
    "v_rcp_f32_e32 v34, v34\n"
    "s_nop 0\n"
    "v_mul_f32_e32 v0, v34, v0\n"
    "v_cvt_pk_bf16_f32 v0, 0, v0\n"
    "v_mul_f32_e32 v0, v1, v0\n"
    "s_nop 1\n"
    "v_mov_b32_dpp v1, v0 quad_perm:[1,0,3,2] row_mask:0xf bank_mask:0xf bound_ctrl:1\n"
    "v_cvt_pk_bf16_f32 v0, v0, v1\n"
    "v_mov_b32_e32 v1, v0\n"
    "s_nop 0\n"
    "v_mov_b32_dpp v0, v0 row_shl:4 row_mask:0xf bank_mask:0x5\n"
    "v_mov_b32_dpp v1, v1 row_shr:4 row_mask:0xf bank_mask:0xa\n"
    "s_nop 0\n"
    "v_mov_b32_dpp v35, v0 quad_perm:[0,0,0,0] row_mask:0xf bank_mask:0xf bound_ctrl:1\n"
    "v_mov_b32_dpp v34, v1 quad_perm:[0,0,0,0] row_mask:0xf bank_mask:0xf bound_ctrl:1\n"
    "v_mov_b32_dpp v1, v1 quad_perm:[2,2,2,2] row_mask:0xf bank_mask:0xf bound_ctrl:1\n"
    "v_dot2c_f32_bf16_e32 v113, v2, v34\n"
    "v_dot2c_f32_bf16_e32 v112, v10, v34\n"
    "v_dot2c_f32_bf16_e32 v111, v3, v34\n"
    "v_dot2c_f32_bf16_e32 v110, v11, v34\n"
    "v_dot2c_f32_bf16_e32 v116, v4, v34\n"
    "v_dot2c_f32_bf16_e32 v115, v12, v34\n"
    "v_dot2c_f32_bf16_e32 v114, v5, v34\n"
    "v_dot2c_f32_bf16_e32 v103, v13, v34\n"
    "v_dot2c_f32_bf16_e32 v113, v6, v1\n"
    "v_dot2c_f32_bf16_e32 v112, v18, v1\n"
    "v_dot2c_f32_bf16_e32 v111, v7, v1\n"
    "v_dot2c_f32_bf16_e32 v110, v19, v1\n"
    "v_dot2c_f32_bf16_e32 v116, v8, v1\n"
    "v_dot2c_f32_bf16_e32 v115, v20, v1\n"
    "v_dot2c_f32_bf16_e32 v114, v9, v1\n"
    "v_dot2c_f32_bf16_e32 v103, v21, v1\n"
    "v_mov_b32_dpp v0, v0 quad_perm:[2,2,2,2] row_mask:0xf bank_mask:0xf bound_ctrl:1\n"
    "v_dot2c_f32_bf16_e32 v113, v14, v35\n"
    "v_dot2c_f32_bf16_e32 v112, v26, v35\n"
    "v_dot2c_f32_bf16_e32 v111, v15, v35\n"
    "v_dot2c_f32_bf16_e32 v110, v27, v35\n"
    "v_dot2c_f32_bf16_e32 v116, v16, v35\n"
    "v_dot2c_f32_bf16_e32 v115, v28, v35\n"
    "v_dot2c_f32_bf16_e32 v114, v17, v35\n"
    "v_dot2c_f32_bf16_e32 v103, v29, v35\n"
    "v_dot2c_f32_bf16_e32 v113, v22, v0\n"
    "v_dot2c_f32_bf16_e32 v112, v30, v0\n"
    "v_dot2c_f32_bf16_e32 v111, v23, v0\n"
    "v_dot2c_f32_bf16_e32 v110, v31, v0\n"
    "v_dot2c_f32_bf16_e32 v116, v24, v0\n"
    "v_dot2c_f32_bf16_e32 v115, v32, v0\n"
    "v_dot2c_f32_bf16_e32 v114, v25, v0\n"
    "v_dot2c_f32_bf16_e32 v103, v33, v0\n"
    "v_permlane32_swap_b32_e32 v113, v116\n"
    "v_permlane32_swap_b32_e32 v112, v115\n"
    "v_permlane32_swap_b32_e32 v111, v114\n"
    "v_permlane32_swap_b32_e32 v110, v103\n"
    "v_add_f32_e32 v0, v113, v116\n"
    "v_add_f32_e32 v1, v112, v115\n"
    "v_add_f32_e32 v2, v111, v114\n"
    "v_add_f32_e32 v3, v110, v103\n"
    "s_nop 0\n"
    "v_permlane16_swap_b32_e32 v0, v2\n"
    "v_permlane16_swap_b32_e32 v1, v3\n"
    "v_pk_add_f32 v[0:1], v[0:1], v[2:3]\n"
    "s_nop 0\n"
    "v_min_u32_e32 v1, -2, v1\n"
    "v_min_u32_e32 v0, -2, v0\n"
    "buffer_store_dwordx2 v[0:1], v102, s[4:7], 0 offen sc1\n"
    ".Lfb_role_b_3:\n"
    "s_endpgm\n"
    ".Lfb_role_b_4:\n"
    "s_mov_b32 s0, 0x20000\n"
    "1:\n"
    "buffer_load_dwordx4 v[224:227], v128, s[4:7], 0 offen sc1\n"
    "buffer_load_dwordx4 v[228:231], v128, s[4:7], s2 offen sc1\n"
    "buffer_load_dwordx4 v[232:235], v128, s[4:7], s3 offen sc1\n"
    "buffer_load_dwordx4 v[236:239], v128, s[4:7], s8 offen sc1\n"
    "buffer_load_dwordx4 v[240:243], v128, s[4:7], s9 offen sc1\n"
    "buffer_load_dwordx4 v[244:247], v128, s[4:7], s10 offen sc1\n"
    "buffer_load_dwordx4 v[248:251], v128, s[4:7], s11 offen sc1\n"
    "buffer_load_dwordx4 v[252:255], v128, s[4:7], s12 offen sc1\n"
    "s_waitcnt vmcnt(0)\n"
    "v_max3_u32 v0, v224, v225, v226\n"
    "v_max3_u32 v0, v0, v227, v228\n"
    "v_max3_u32 v0, v0, v229, v230\n"
    "v_max3_u32 v0, v0, v231, v232\n"
    "v_max3_u32 v0, v0, v233, v234\n"
    "v_max3_u32 v0, v0, v235, v236\n"
    "v_max3_u32 v0, v0, v237, v238\n"
    "v_max3_u32 v0, v0, v239, v240\n"
    "v_max3_u32 v0, v0, v241, v242\n"
    "v_max3_u32 v0, v0, v243, v244\n"
    "v_max3_u32 v0, v0, v245, v246\n"
    "v_max3_u32 v0, v0, v247, v248\n"
    "v_max3_u32 v0, v0, v249, v250\n"
    "v_max3_u32 v0, v0, v251, v252\n"
    "v_max3_u32 v0, v0, v253, v254\n"
    "v_max3_u32 v0, v0, v255, v255\n"
    "v_cmp_eq_u32 vcc, -1, v0\n"
    "s_cbranch_vccz 2f\n"
    "s_mov_b64 exec, vcc\n"
    "s_sub_u32 s0, s0, 1\n"
    "s_cbranch_scc0 1b\n"
    "2:\n"
    "s_mov_b64 exec, -1\n"
    "s_nop 0\n"
    "v_pk_add_f32 v[0:1], v[226:227], v[230:231]\n"
    "v_pk_add_f32 v[108:109], v[224:225], v[228:229]\n"
    "v_pk_add_f32 v[128:129], v[234:235], v[238:239]\n"
    "v_pk_add_f32 v[130:131], v[232:233], v[236:237]\n"
    "v_pk_add_f32 v[0:1], v[0:1], v[128:129]\n"
    "v_pk_add_f32 v[108:109], v[108:109], v[130:131]\n"
    "v_pk_add_f32 v[128:129], v[242:243], v[246:247]\n"
    "v_pk_add_f32 v[130:131], v[240:241], v[244:245]\n"
    "v_pk_add_f32 v[132:133], v[250:251], v[254:255]\n"
    "v_pk_add_f32 v[134:135], v[248:249], v[252:253]\n"
    "v_pk_add_f32 v[128:129], v[128:129], v[132:133]\n"
    "v_pk_add_f32 v[130:131], v[130:131], v[134:135]\n"
    "v_pk_add_f32 v[0:1], v[0:1], v[128:129]\n"
    "v_pk_add_f32 v[108:109], v[108:109], v[130:131]\n"
    "s_branch .Lfb_role_b_2\n"
    ".p2align 6\n"
    ".Lfb_role_bh:\n"
    "s_load_dwordx2 s[4:5], s[0:1], 0x58\n"
    "s_ashr_i32 s11, s2, 3\n"
    "v_and_b32_e32 v0, 63, v0\n"
    "s_mov_b32 s7, 0x20000\n"
    "s_mov_b32 s6, 0x83400\n"
    "s_waitcnt lgkmcnt(0)\n"
    "s_load_dword s10, s[4:5], 0x0\n"
    "s_and_b32 s3, s5, 0xffff\n"
    "s_cmp_lt_i32 s11, 15\n"
    "s_cselect_b64 s[0:1], -1, 0\n"
    "s_and_b64 vcc, exec, s[0:1]\n"
    "v_lshlrev_b32_e32 v1, 4, v0\n"
    "s_cbranch_vccnz .Lfb_role_bh_2\n"
    "v_lshlrev_b32_e32 v2, 4, v0\n"
    "s_and_b32 s2, s2, 7\n"
    "s_add_i32 s8, s11, -8\n"
    "s_cbranch_execz .Lfb_role_bh_3\n"
    "s_branch .Lfb_role_bh_4\n"
    ".Lfb_role_bh_2:\n"
    "s_and_b32 s2, s2, 7\n"
    "s_add_i32 s8, s11, -8\n"
    ".Lfb_role_bh_3:\n"
    "s_mul_i32 s5, s2, 7\n"
    "s_mov_b32 s12, -1\n"
    "s_add_i32 s5, s8, s5\n"
    "s_mov_b32 s13, s12\n"
    "v_lshl_or_b32 v2, s5, 11, v1\n"
    "s_mov_b32 s14, s12\n"
    "s_mov_b32 s15, s12\n"
    "v_mov_b64_e32 v[4:5], s[12:13]\n"
    "v_add_u32_e32 v2, 0x63000, v2\n"
    "s_mov_b32 s5, s3\n"
    "v_mov_b64_e32 v[6:7], s[14:15]\n"
    "buffer_store_dwordx4 v[4:7], v2, s[4:7], 0 offen offset:1024 sc1\n"
    "buffer_store_dwordx4 v[4:7], v2, s[4:7], 0 offen offset:2048 sc1\n"
    "v_mov_b32_e32 v2, v1\n"
    ".Lfb_role_bh_4:\n"
    "s_lshl_b32 s2, s2, 10\n"
    "s_lshl_b32 s5, s8, 13\n"
    "s_or_b32 s2, s2, s5\n"
    "s_addk_i32 s2, 0x1000\n"
    "v_add_u32_e32 v1, s2, v2\n"
    "v_mov_b32_e32 v2, 0x800\n"
    "v_lshl_or_b32 v6, v0, 3, v2\n"
    "s_waitcnt lgkmcnt(0)\n"
    "s_waitcnt vmcnt(0)\n"
    "s_waitcnt lgkmcnt(0)\n"
    "s_barrier\n"
    "ds_read2st64_b64 v[2:5], v6 offset1:1\n"
    "ds_read2st64_b64 v[6:9], v6 offset0:2 offset1:3\n"
    "s_add_i32 s2, s10, 1\n"
    "s_mov_b32 s5, s3\n"
    "s_andn2_b64 vcc, exec, s[0:1]\n"
    "s_waitcnt lgkmcnt(1)\n"
    "v_pk_add_f32 v[2:3], v[2:3], v[4:5]\n"
    "s_waitcnt lgkmcnt(0)\n"
    "v_pk_add_f32 v[4:5], v[6:7], v[8:9]\n"
    "s_nop 0\n"
    "v_pk_add_f32 v[4:5], v[2:3], v[4:5]\n"
    "v_mov_b32_e32 v3, s2\n"
    "v_mov_b32_e32 v2, v4\n"
    "v_mov_b32_e32 v4, v5\n"
    "v_mov_b32_e32 v5, s2\n"
    "buffer_store_dwordx4 v[2:5], v1, s[4:7], 0 offen sc1\n"
    "s_waitcnt lgkmcnt(0)\n"
    "s_barrier\n"
    "s_cbranch_vccnz .Lfb_role_bh_6\n"
    "v_lshlrev_b32_e32 v0, 7, v0\n"
    "s_sleep 0x78\n"
    "v_add_u32_e32 v1, 0x11000, v0\n"
    "s_mov_b32 s0, 0x20000\n"
    "1:\n"
    "buffer_load_dword v2, v1, s[4:7], 0 offen sc1\n"
    "s_waitcnt vmcnt(0)\n"
    "v_cmp_ne_u32 vcc, s2, v2\n"
    "s_cbranch_vccz 2f\n"
    "s_mov_b64 exec, vcc\n"
    "s_sub_u32 s0, s0, 1\n"
    "s_cbranch_scc0 1b\n"
    "2:\n"
    "s_mov_b64 exec, -1\n"
    "s_waitcnt lgkmcnt(0)\n"
    "s_barrier\n"
    "v_and_b32_e32 v0, 0x380, v0\n"
    "v_or_b32_e32 v0, 0x17000, v0\n"
    "s_mov_b32 s0, 0x20000\n"
    "1:\n"
    "buffer_load_dword v1, v0, s[4:7], 0 offen sc1\n"
    "s_waitcnt vmcnt(0)\n"
    "v_cmp_ne_u32 vcc, s2, v1\n"
    "s_cbranch_vccz 2f\n"
    "s_mov_b64 exec, vcc\n"
    "s_sub_u32 s0, s0, 1\n"
    "s_cbranch_scc0 1b\n"
    "2:\n"
    "s_mov_b64 exec, -1\n"
    "s_sleep 14\n"
    "s_waitcnt lgkmcnt(0)\n"
    "s_barrier\n"
    ".Lfb_role_bh_6:\n"
    "s_endpgm\n"
    ".p2align 6\n"
    ".Lfb_role_q_tail:\n"
    "s_load_dwordx2 s[8:9], s[0:1], 0x58\n"
    "s_load_dwordx2 s[4:5], s[0:1], 0x48\n"
    "s_mov_b32 s2, 0xf800000\n"
    "s_mul_i32 s22, s12, 0x9800\n"
    "s_lshl_b32 s17, s17, 8\n"
    "s_waitcnt lgkmcnt(0)\n"
    "s_load_dword s16, s[8:9], 0x0\n"
    "s_waitcnt vmcnt(14)\n"
    "v_mov_b32_e32 v80, 0\n"
    "v_lshlrev_b32_e32 v74, 16, v66\n"
    "v_and_b32_e32 v66, 0xffff0000, v66\n"
    "v_lshlrev_b32_e32 v75, 16, v67\n"
    "v_and_b32_e32 v67, 0xffff0000, v67\n"
    "v_add_f32_e32 v77, v74, v66\n"
    "v_lshlrev_b32_e32 v76, 16, v68\n"
    "v_add_f32_e32 v78, v75, v67\n"
    "v_add_f32_e32 v77, 0, v77\n"
    "v_and_b32_e32 v68, 0xffff0000, v68\n"
    "v_add_f32_e32 v77, v78, v77\n"
    "v_add_f32_e32 v78, v76, v68\n"
    "v_add_f32_e32 v77, v78, v77\n"
    "v_lshlrev_b32_e32 v78, 16, v69\n"
    "v_and_b32_e32 v69, 0xffff0000, v69\n"
    "v_add_f32_e32 v79, v78, v69\n"
    "v_add_f32_e32 v77, v79, v77\n"
    "v_mov_b32_e32 v81, 0\n"
    "s_movk_i32 s6, 0x1000\n"
    "v_add_f32_dpp v77, v77, v77 quad_perm:[1,0,3,2] row_mask:0xf bank_mask:0xf bound_ctrl:1\n"
    "s_and_b32 s9, s9, 0xffff\n"
    "s_mov_b32 s10, 0x83400\n"
    "v_add_f32_dpp v77, v77, v77 quad_perm:[2,3,0,1] row_mask:0xf bank_mask:0xf bound_ctrl:1\n"
    "s_mov_b32 s11, s7\n"
    "s_andn2_b32 s13, s13, 63\n"
    "v_add_f32_dpp v77, v77, v77 row_half_mirror row_mask:0xf bank_mask:0xf bound_ctrl:1\n"
    "s_and_b32 s5, s5, 0xffff\n"
    "s_nop 0\n"
    "v_add_f32_dpp v77, v77, v77 row_ror:8 row_mask:0xf bank_mask:0xf bound_ctrl:1\n"
    "v_mov_b32_e32 v79, v77\n"
    "s_nop 1\n"
    "v_permlane16_swap_b32_e32 v77, v79\n"
    "v_add_f32_e32 v77, v77, v79\n"
    "v_mov_b32_e32 v79, v77\n"
    "s_nop 1\n"
    "v_permlane32_swap_b32_e32 v77, v79\n"
    "v_add_f32_e32 v77, v77, v79\n"
    "v_fmac_f32_e32 v66, 0xbb000000, v77\n"
    "v_fmac_f32_e32 v67, 0xbb000000, v77\n"
    "v_fmac_f32_e32 v74, 0xbb000000, v77\n"
    "v_mul_f32_e32 v66, v66, v66\n"
    "v_fmac_f32_e32 v75, 0xbb000000, v77\n"
    "v_mul_f32_e32 v67, v67, v67\n"
    "v_fmac_f32_e32 v66, v74, v74\n"
    "v_fmac_f32_e32 v67, v75, v75\n"
    "v_fmac_f32_e32 v68, 0xbb000000, v77\n"
    "v_add_f32_e32 v66, v66, v67\n"
    "v_fmac_f32_e32 v76, 0xbb000000, v77\n"
    "v_mul_f32_e32 v67, v68, v68\n"
    "v_fmac_f32_e32 v67, v76, v76\n"
    "v_fmac_f32_e32 v69, 0xbb000000, v77\n"
    "v_add_f32_e32 v66, v67, v66\n"
    "v_fmac_f32_e32 v78, 0xbb000000, v77\n"
    "v_mul_f32_e32 v67, v69, v69\n"
    "v_fmac_f32_e32 v67, v78, v78\n"
    "v_add_f32_e32 v66, v67, v66\n"
    "v_mul_f32_e32 v68, 0x3b000000, v77\n"
    "v_cvt_pk_bf16_f32 v68, 0, v68\n"
    "v_add_f32_dpp v66, v66, v66 quad_perm:[1,0,3,2] row_mask:0xf bank_mask:0xf bound_ctrl:1\n"
    "v_mov_b32_e32 v76, 0\n"
    "v_mov_b32_e32 v77, 0\n"
    "v_add_f32_dpp v66, v66, v66 quad_perm:[2,3,0,1] row_mask:0xf bank_mask:0xf bound_ctrl:1\n"
    "v_mov_b32_e32 v78, 0\n"
    "v_mov_b32_e32 v79, 0\n"
    "v_add_f32_dpp v66, v66, v66 row_half_mirror row_mask:0xf bank_mask:0xf bound_ctrl:1\n"
    "s_nop 1\n"
    "v_add_f32_dpp v66, v66, v66 row_ror:8 row_mask:0xf bank_mask:0xf bound_ctrl:1\n"
    "v_mov_b32_e32 v67, v66\n"
    "s_nop 1\n"
    "v_permlane16_swap_b32_e32 v66, v67\n"
    "v_add_f32_e32 v66, v66, v67\n"
    "v_mov_b32_e32 v67, v66\n"
    "s_nop 1\n"
    "v_permlane32_swap_b32_e32 v66, v67\n"
    "v_add_f32_e32 v66, v66, v67\n"
    "v_mul_f32_e32 v66, 0x3b000000, v66\n"
    "v_cvt_pk_bf16_f32 v66, 0, v66\n"
    "v_add_f32_e32 v66, 0x3727c5ac, v66\n"
    "v_cvt_pk_bf16_f32 v66, 0, v66\n"
    "v_mul_f32_e32 v67, 0x4f800000, v66\n"
    "v_cmp_gt_f32_e32 vcc, s2, v66\n"
    "s_nop 1\n"
    "v_cndmask_b32_e32 v66, v66, v67, vcc\n"
    "v_sqrt_f32_e32 v67, v66\n"
    "s_nop 0\n"
    "v_add_u32_e32 v69, -1, v67\n"
    "v_fma_f32 v74, -v69, v67, v66\n"
    "v_cmp_ge_f32_e64 s[2:3], 0, v74\n"
    "v_add_u32_e32 v74, 1, v67\n"
    "s_nop 0\n"
    "v_cndmask_b32_e64 v69, v67, v69, s[2:3]\n"
    "v_fma_f32 v67, -v74, v67, v66\n"
    "v_cmp_lt_f32_e64 s[2:3], 0, v67\n"
    "s_nop 1\n"
    "v_cndmask_b32_e64 v67, v69, v74, s[2:3]\n"
    "v_mul_f32_e32 v69, 0x37800000, v67\n"
    "v_cndmask_b32_e32 v67, v67, v69, vcc\n"
    "v_mov_b32_e32 v69, 0x260\n"
    "v_cmp_class_f32_e32 vcc, v66, v69\n"
    "s_nop 1\n"
    "v_cndmask_b32_e32 v66, v67, v66, vcc\n"
    "v_lshlrev_b32_e32 v67, 16, v73\n"
    "v_sub_f32_e32 v67, v67, v68\n"
    "v_cvt_pk_bf16_f32 v66, 0, v66\n"
    "v_cvt_pk_bf16_f32 v67, 0, v67\n"
    "v_div_scale_f32 v68, s[2:3], v66, v66, v67\n"
    "v_rcp_f32_e32 v69, v68\n"
    "s_movk_i32 s2, 0xc1e\n"
    "s_mov_b32 s3, 0x7060302\n"
    "v_fma_f32 v73, -v68, v69, 1.0\n"
    "v_fmac_f32_e32 v69, v73, v69\n"
    "v_div_scale_f32 v73, vcc, v67, v66, v67\n"
    "v_mul_f32_e32 v74, v73, v69\n"
    "v_fma_f32 v75, -v68, v74, v73\n"
    "v_fmac_f32_e32 v74, v75, v69\n"
    "v_fma_f32 v68, -v68, v74, v73\n"
    "v_div_fmas_f32 v68, v68, v69, v74\n"
    "v_div_fixup_f32 v66, v68, v66, v67\n"
    "v_cvt_pk_bf16_f32 v73, 0, v66\n"
    "v_lshlrev_b32_e32 v66, 6, v0\n"
    "v_bitop3_b32 v66, v72, s2, v66 bitop3:0xc8\n"
    "v_or_b32_e32 v66, s14, v66\n"
    "s_lshl_b32 s2, s20, 12\n"
    "v_add_u32_e32 v66, s21, v66\n"
    "v_mov_b32_e32 v67, 0x1000\n"
    "s_add_i32 s2, s22, s2\n"
    "v_lshl_add_u32 v72, v66, 3, v67\n"
    "v_add_u32_e32 v66, s17, v0\n"
    "s_add_i32 s2, s2, 0x1cc00\n"
    "v_lshl_add_u32 v82, v66, 3, s2\n"
    "v_lshlrev_b32_e32 v66, 4, v70\n"
    "v_lshl_or_b32 v66, v71, 5, v66\n"
    "v_lshl_or_b32 v66, s19, 10, v66\n"
    "v_mov_b32_e32 v74, 0\n"
    "v_mov_b32_e32 v75, 0\n"
    "v_add_u32_e32 v70, 0x1000, v66\n"
    "v_lshl_or_b32 v71, v0, 2, v67\n"
    "s_mov_b32 s19, 0x9000\n"
    "s_waitcnt lgkmcnt(0)\n"
    "s_waitcnt vmcnt(8)\n"
    "s_mov_b32 s2, 0x5040100\n"
    "v_perm_b32 v66, v62, v58, s2\n"
    "v_perm_b32 v67, v63, v59, s2\n"
    "v_perm_b32 v68, v64, v60, s2\n"
    "v_perm_b32 v69, v65, v61, s2\n"
    "v_perm_b32 v62, v62, v58, s3\n"
    "v_perm_b32 v63, v63, v59, s3\n"
    "v_perm_b32 v64, v64, v60, s3\n"
    "v_perm_b32 v65, v65, v61, s3\n"
    "v_perm_b32 v58, v54, v50, s2\n"
    "v_perm_b32 v59, v55, v51, s2\n"
    "v_perm_b32 v60, v56, v52, s2\n"
    "v_perm_b32 v61, v57, v53, s2\n"
    "v_perm_b32 v50, v54, v50, s3\n"
    "v_perm_b32 v51, v55, v51, s3\n"
    "v_perm_b32 v52, v56, v52, s3\n"
    "v_perm_b32 v53, v57, v53, s3\n"
    "v_perm_b32 v54, v46, v42, s2\n"
    "v_perm_b32 v55, v47, v43, s2\n"
    "v_perm_b32 v56, v48, v44, s2\n"
    "v_perm_b32 v57, v49, v45, s2\n"
    "v_perm_b32 v42, v46, v42, s3\n"
    "v_perm_b32 v43, v47, v43, s3\n"
    "v_perm_b32 v44, v48, v44, s3\n"
    "v_perm_b32 v45, v49, v45, s3\n"
    "v_perm_b32 v46, v38, v34, s2\n"
    "v_perm_b32 v47, v39, v35, s2\n"
    "v_perm_b32 v48, v40, v36, s2\n"
    "v_perm_b32 v49, v41, v37, s2\n"
    "v_perm_b32 v34, v38, v34, s3\n"
    "v_perm_b32 v35, v39, v35, s3\n"
    "v_perm_b32 v36, v40, v36, s3\n"
    "v_perm_b32 v37, v41, v37, s3\n"
    "buffer_load_dwordx4 v[224:227], v72, s[8:11], 0 offen sc1\n"
    "buffer_load_dwordx4 v[228:231], v72, s[8:11], s18 offen sc1\n"
    "buffer_load_dwordx4 v[232:235], v72, s[8:11], s6 offen sc1\n"
    "buffer_load_dwordx4 v[236:239], v72, s[8:11], s19 offen sc1\n"
    "s_waitcnt vmcnt(6)\n"
    "s_add_i32 s16, s16, 1\n"
    "v_perm_b32 v38, v30, v26, s2\n"
    "v_perm_b32 v39, v31, v27, s2\n"
    "v_perm_b32 v40, v32, v28, s2\n"
    "v_perm_b32 v41, v33, v29, s2\n"
    "v_perm_b32 v26, v30, v26, s3\n"
    "v_perm_b32 v27, v31, v27, s3\n"
    "v_perm_b32 v28, v32, v28, s3\n"
    "v_perm_b32 v29, v33, v29, s3\n"
    "v_perm_b32 v30, v22, v18, s2\n"
    "v_perm_b32 v31, v23, v19, s2\n"
    "v_perm_b32 v32, v24, v20, s2\n"
    "v_perm_b32 v33, v25, v21, s2\n"
    "v_perm_b32 v18, v22, v18, s3\n"
    "v_perm_b32 v19, v23, v19, s3\n"
    "v_perm_b32 v20, v24, v20, s3\n"
    "v_perm_b32 v21, v25, v21, s3\n"
    "v_perm_b32 v22, v14, v10, s2\n"
    "v_perm_b32 v23, v15, v11, s2\n"
    "v_perm_b32 v24, v16, v12, s2\n"
    "v_perm_b32 v25, v17, v13, s2\n"
    "v_perm_b32 v10, v14, v10, s3\n"
    "v_perm_b32 v11, v15, v11, s3\n"
    "v_perm_b32 v12, v16, v12, s3\n"
    "v_perm_b32 v13, v17, v13, s3\n"
    "s_waitcnt vmcnt(4)\n"
    "s_nop 0\n"
    "v_perm_b32 v14, v6, v2, s2\n"
    "v_perm_b32 v15, v7, v3, s2\n"
    "v_perm_b32 v16, v8, v4, s2\n"
    "v_perm_b32 v17, v9, v5, s2\n"
    "v_perm_b32 v2, v6, v2, s3\n"
    "v_perm_b32 v3, v7, v3, s3\n"
    "v_perm_b32 v4, v8, v4, s3\n"
    "v_perm_b32 v5, v9, v5, s3\n"
    "s_mov_b32 s20, 0x20000\n"
    "s_branch 3f\n"
    "1:\n"
    "s_sleep 2\n"
    "buffer_load_dwordx4 v[224:227], v72, s[8:11], 0 offen sc1\n"
    "buffer_load_dwordx4 v[228:231], v72, s[8:11], s18 offen sc1\n"
    "buffer_load_dwordx4 v[232:235], v72, s[8:11], s6 offen sc1\n"
    "buffer_load_dwordx4 v[236:239], v72, s[8:11], s19 offen sc1\n"
    "3:\n"
    "s_waitcnt vmcnt(0)\n"
    "v_xor_b32 v6, s16, v225\n"
    "v_bitop3_b32 v6, v227, v6, s16 bitop3:0xde\n"
    "v_bitop3_b32 v6, v229, v6, s16 bitop3:0xde\n"
    "v_bitop3_b32 v6, v231, v6, s16 bitop3:0xde\n"
    "v_bitop3_b32 v6, v233, v6, s16 bitop3:0xde\n"
    "v_bitop3_b32 v6, v235, v6, s16 bitop3:0xde\n"
    "v_bitop3_b32 v6, v237, v6, s16 bitop3:0xde\n"
    "v_bitop3_b32 v6, v239, v6, s16 bitop3:0xde\n"
    "v_cmp_ne_u32 vcc, 0, v6\n"
    "s_cbranch_vccz 2f\n"
    "s_mov_b64 exec, vcc\n"
    "s_sub_u32 s20, s20, 1\n"
    "s_cbranch_scc0 1b\n"
    "2:\n"
    "s_mov_b64 exec, -1\n"
    "s_mov_b32 s6, 0x300000\n"
    "v_add_f32_e32 v6, v224, v228\n"
    "v_add_f32_e32 v7, v226, v230\n"
    "s_nop 1\n"
    "v_permlane32_swap_b32_e32 v6, v7\n"
    "v_add_f32_e32 v6, v6, v7\n"
    "v_add_f32_e32 v7, v232, v236\n"
    "v_add_f32_e32 v8, v234, v238\n"
    "s_nop 1\n"
    "v_permlane32_swap_b32_e32 v7, v8\n"
    "v_add_f32_e32 v7, v7, v8\n"
    "v_mov_b32_e32 v8, v6\n"
    "s_nop 1\n"
    "v_permlane16_swap_b32_e32 v6, v8\n"
    "v_add_f32_e32 v6, v6, v8\n"
    "v_mov_b32_e32 v8, v7\n"
    "s_nop 1\n"
    "v_permlane16_swap_b32_e32 v7, v8\n"
    "v_add_f32_e32 v7, v7, v8\n"
    "v_cvt_pk_bf16_f32 v7, 0, v7\n"
    "v_add_f32_e32 v7, 1.0, v7\n"
    "v_cvt_pk_bf16_f32 v7, 0, v7\n"
    "v_mul_f32_e32 v7, v73, v7\n"
    "v_cvt_pk_bf16_f32 v6, 0, v6\n"
    "v_cvt_pk_bf16_f32 v7, 0, v7\n"
    "v_add_f32_e32 v6, v6, v7\n"
    "s_mov_b32 s18, 0x9000\n"
    "s_nop 0\n"
    "v_mov_b32_dpp v7, v6 quad_perm:[1,0,3,2] row_mask:0xf bank_mask:0xf bound_ctrl:1\n"
    "v_cvt_pk_bf16_f32 v6, v6, v7\n"
    "s_nop 1\n"
    "v_mov_b32_dpp v7, v6 row_newbcast:0 row_mask:0xf bank_mask:0xf bound_ctrl:1\n"
    "v_mov_b32_dpp v8, v6 row_newbcast:2 row_mask:0xf bank_mask:0xf bound_ctrl:1\n"
    "v_dot2c_f32_bf16_e32 v74, v66, v7\n"
    "v_dot2c_f32_bf16_e32 v75, v62, v7\n"
    "v_dot2c_f32_bf16_e32 v76, v67, v7\n"
    "v_dot2c_f32_bf16_e32 v77, v63, v7\n"
    "v_dot2c_f32_bf16_e32 v78, v68, v7\n"
    "v_dot2c_f32_bf16_e32 v79, v64, v7\n"
    "v_dot2c_f32_bf16_e32 v80, v69, v7\n"
    "v_dot2c_f32_bf16_e32 v81, v65, v7\n"
    "v_mov_b32_dpp v9, v6 row_newbcast:4 row_mask:0xf bank_mask:0xf bound_ctrl:1\n"
    "v_dot2c_f32_bf16_e32 v74, v58, v8\n"
    "v_dot2c_f32_bf16_e32 v75, v50, v8\n"
    "v_dot2c_f32_bf16_e32 v76, v59, v8\n"
    "v_dot2c_f32_bf16_e32 v77, v51, v8\n"
    "v_dot2c_f32_bf16_e32 v78, v60, v8\n"
    "v_dot2c_f32_bf16_e32 v79, v52, v8\n"
    "v_dot2c_f32_bf16_e32 v80, v61, v8\n"
    "v_dot2c_f32_bf16_e32 v81, v53, v8\n"
    "v_mov_b32_dpp v72, v6 row_newbcast:6 row_mask:0xf bank_mask:0xf bound_ctrl:1\n"
    "v_dot2c_f32_bf16_e32 v74, v54, v9\n"
    "v_dot2c_f32_bf16_e32 v75, v42, v9\n"
    "v_dot2c_f32_bf16_e32 v76, v55, v9\n"
    "v_dot2c_f32_bf16_e32 v77, v43, v9\n"
    "v_dot2c_f32_bf16_e32 v78, v56, v9\n"
    "v_dot2c_f32_bf16_e32 v79, v44, v9\n"
    "v_dot2c_f32_bf16_e32 v80, v57, v9\n"
    "v_dot2c_f32_bf16_e32 v81, v45, v9\n"
    "v_mov_b32_dpp v73, v6 row_newbcast:8 row_mask:0xf bank_mask:0xf bound_ctrl:1\n"
    "v_dot2c_f32_bf16_e32 v74, v46, v72\n"
    "v_dot2c_f32_bf16_e32 v75, v34, v72\n"
    "v_dot2c_f32_bf16_e32 v76, v47, v72\n"
    "v_dot2c_f32_bf16_e32 v77, v35, v72\n"
    "v_dot2c_f32_bf16_e32 v78, v48, v72\n"
    "v_dot2c_f32_bf16_e32 v79, v36, v72\n"
    "v_dot2c_f32_bf16_e32 v80, v49, v72\n"
    "v_dot2c_f32_bf16_e32 v81, v37, v72\n"
    "v_mov_b32_dpp v83, v6 row_newbcast:10 row_mask:0xf bank_mask:0xf bound_ctrl:1\n"
    "v_dot2c_f32_bf16_e32 v74, v38, v73\n"
    "v_dot2c_f32_bf16_e32 v75, v26, v73\n"
    "v_dot2c_f32_bf16_e32 v76, v39, v73\n"
    "v_dot2c_f32_bf16_e32 v77, v27, v73\n"
    "v_dot2c_f32_bf16_e32 v78, v40, v73\n"
    "v_dot2c_f32_bf16_e32 v79, v28, v73\n"
    "v_dot2c_f32_bf16_e32 v80, v41, v73\n"
    "v_dot2c_f32_bf16_e32 v81, v29, v73\n"
    "v_mov_b32_dpp v84, v6 row_newbcast:12 row_mask:0xf bank_mask:0xf bound_ctrl:1\n"
    "v_dot2c_f32_bf16_e32 v74, v30, v83\n"
    "v_dot2c_f32_bf16_e32 v75, v18, v83\n"
    "v_dot2c_f32_bf16_e32 v76, v31, v83\n"
    "v_dot2c_f32_bf16_e32 v77, v19, v83\n"
    "v_dot2c_f32_bf16_e32 v78, v32, v83\n"
    "v_dot2c_f32_bf16_e32 v79, v20, v83\n"
    "v_dot2c_f32_bf16_e32 v80, v33, v83\n"
    "v_dot2c_f32_bf16_e32 v81, v21, v83\n"
    "v_mov_b32_dpp v6, v6 row_newbcast:14 row_mask:0xf bank_mask:0xf bound_ctrl:1\n"
    "v_dot2c_f32_bf16_e32 v74, v22, v84\n"
    "v_dot2c_f32_bf16_e32 v75, v10, v84\n"
    "v_dot2c_f32_bf16_e32 v76, v23, v84\n"
    "v_dot2c_f32_bf16_e32 v77, v11, v84\n"
    "v_dot2c_f32_bf16_e32 v78, v24, v84\n"
    "v_dot2c_f32_bf16_e32 v79, v12, v84\n"
    "v_dot2c_f32_bf16_e32 v80, v25, v84\n"
    "v_dot2c_f32_bf16_e32 v81, v13, v84\n"
    "v_dot2c_f32_bf16_e32 v74, v14, v6\n"
    "v_dot2c_f32_bf16_e32 v75, v2, v6\n"
    "v_dot2c_f32_bf16_e32 v76, v15, v6\n"
    "v_dot2c_f32_bf16_e32 v77, v3, v6\n"
    "v_dot2c_f32_bf16_e32 v78, v16, v6\n"
    "v_dot2c_f32_bf16_e32 v79, v4, v6\n"
    "v_dot2c_f32_bf16_e32 v80, v17, v6\n"
    "v_dot2c_f32_bf16_e32 v81, v5, v6\n"
    "v_permlane32_swap_b32_e32 v74, v78\n"
    "v_permlane32_swap_b32_e32 v75, v79\n"
    "v_permlane32_swap_b32_e32 v76, v80\n"
    "v_permlane32_swap_b32_e32 v77, v81\n"
    "v_add_f32_e32 v2, v74, v78\n"
    "v_add_f32_e32 v3, v75, v79\n"
    "v_add_f32_e32 v4, v76, v80\n"
    "v_add_f32_e32 v5, v77, v81\n"
    "ds_write_b128 v70, v[2:5]\n"
    "s_waitcnt lgkmcnt(0)\n"
    "s_barrier\n"
    "ds_read2st64_b32 v[2:3], v71 offset1:4\n"
    "ds_read2st64_b32 v[4:5], v71 offset0:8 offset1:12\n"
    "v_mov_b32_e32 v42, 0\n"
    "s_waitcnt lgkmcnt(1)\n"
    "v_add_f32_e32 v2, v2, v3\n"
    "s_waitcnt lgkmcnt(0)\n"
    "v_add_f32_e32 v3, v4, v5\n"
    "v_add_f32_e32 v2, v2, v3\n"
    "v_mov_b32_e32 v3, s16\n"
    "buffer_store_dwordx2 v[2:3], v82, s[8:11], 0 offen sc1\n"
    "v_and_or_b32 v2, v0, 56, s13\n"
    "s_waitcnt lgkmcnt(0)\n"
    "s_barrier\n"
    "v_add_u32_e32 v2, s17, v2\n"
    "s_load_dwordx2 s[0:1], s[0:1], 0x40\n"
    "v_lshlrev_b32_e32 v3, 1, v2\n"
    "s_waitcnt lgkmcnt(0)\n"
    "global_load_dwordx4 v[26:29], v3, s[0:1]\n"
    "s_movk_i32 s0, 0x1800\n"
    "v_lshlrev_b32_e32 v3, 4, v1\n"
    "v_mul_lo_u32 v2, v2, s0\n"
    "v_lshl_or_b32 v3, s12, 9, v3\n"
    "v_or3_b32 v2, v3, v2, s14\n"
    "v_add_u32_e32 v6, 0x800, v2\n"
    "buffer_load_dwordx4 v[30:33], v6, s[4:7], s15 offen nt\n"
    "buffer_load_dwordx4 v[34:37], v6, s[4:7], s0 offen nt\n"
    "s_movk_i32 s0, 0x3000\n"
    "buffer_load_dwordx4 v[18:21], v6, s[4:7], s0 offen nt\n"
    "s_movk_i32 s0, 0x4800\n"
    "buffer_load_dwordx4 v[22:25], v6, s[4:7], s0 offen nt\n"
    "s_movk_i32 s0, 0x6000\n"
    "buffer_load_dwordx4 v[10:13], v6, s[4:7], s0 offen nt\n"
    "s_movk_i32 s0, 0x7800\n"
    "buffer_load_dwordx4 v[14:17], v6, s[4:7], s0 offen nt\n"
    "buffer_load_dwordx4 v[2:5], v6, s[4:7], s18 offen nt\n"
    "s_mov_b32 s0, 0xa800\n"
    "buffer_load_dwordx4 v[6:9], v6, s[4:7], s0 offen nt\n"
    "s_waitcnt vmcnt(8)\n"
    "v_bfe_u32 v0, v0, 3, 3\n"
    "v_lshlrev_b32_e32 v38, 16, v26\n"
    "v_mul_f32_e32 v39, 0xbfb8aa3b, v38\n"
    "v_exp_f32_e32 v39, v39\n"
    "v_and_b32_e32 v26, 0xffff0000, v26\n"
    "v_mul_f32_e32 v45, 0xbfb8aa3b, v26\n"
    "v_exp_f32_e32 v45, v45\n"
    "v_add_f32_e32 v39, 1.0, v39\n"
    "v_div_scale_f32 v40, s[0:1], v39, v39, v38\n"
    "v_rcp_f32_e32 v41, v40\n"
    "s_waitcnt vmcnt(0)\n"
    "v_lshlrev_b32_e32 v1, 3, v1\n"
    "v_or3_b32 v0, v1, v0, s13\n"
    "v_fma_f32 v43, -v40, v41, 1.0\n"
    "v_fmac_f32_e32 v41, v43, v41\n"
    "v_div_scale_f32 v43, vcc, v38, v39, v38\n"
    "v_mul_f32_e32 v44, v43, v41\n"
    "v_fma_f32 v46, -v40, v44, v43\n"
    "v_fmac_f32_e32 v44, v46, v41\n"
    "v_fma_f32 v40, -v40, v44, v43\n"
    "v_add_f32_e32 v43, 1.0, v45\n"
    "v_div_scale_f32 v45, s[0:1], v43, v43, v26\n"
    "v_rcp_f32_e32 v46, v45\n"
    "v_div_fmas_f32 v40, v40, v41, v44\n"
    "v_lshlrev_b32_e32 v41, 16, v27\n"
    "v_mul_f32_e32 v44, 0xbfb8aa3b, v41\n"
    "v_div_fixup_f32 v38, v40, v39, v38\n"
    "v_fma_f32 v39, -v45, v46, 1.0\n"
    "v_exp_f32_e32 v44, v44\n"
    "v_fmac_f32_e32 v46, v39, v46\n"
    "v_div_scale_f32 v39, vcc, v26, v43, v26\n"
    "v_mul_f32_e32 v40, v39, v46\n"
    "v_fma_f32 v47, -v45, v40, v39\n"
    "v_fmac_f32_e32 v40, v47, v46\n"
    "v_add_f32_e32 v44, 1.0, v44\n"
    "v_fma_f32 v39, -v45, v40, v39\n"
    "v_div_scale_f32 v45, s[0:1], v44, v44, v41\n"
    "v_rcp_f32_e32 v47, v45\n"
    "v_and_b32_e32 v27, 0xffff0000, v27\n"
    "v_div_fmas_f32 v39, v39, v46, v40\n"
    "v_mul_f32_e32 v40, 0xbfb8aa3b, v27\n"
    "v_div_fixup_f32 v26, v39, v43, v26\n"
    "v_exp_f32_e32 v40, v40\n"
    "v_cvt_pk_bf16_f32 v38, v38, v26\n"
    "v_fma_f32 v26, -v45, v47, 1.0\n"
    "v_fmac_f32_e32 v47, v26, v47\n"
    "v_div_scale_f32 v26, vcc, v41, v44, v41\n"
    "v_mul_f32_e32 v39, v26, v47\n"
    "v_fma_f32 v43, -v45, v39, v26\n"
    "v_add_f32_e32 v40, 1.0, v40\n"
    "v_fmac_f32_e32 v39, v43, v47\n"
    "v_div_scale_f32 v43, s[0:1], v40, v40, v27\n"
    "v_fma_f32 v26, -v45, v39, v26\n"
    "v_rcp_f32_e32 v45, v43\n"
    "v_div_fmas_f32 v26, v26, v47, v39\n"
    "v_div_fixup_f32 v26, v26, v44, v41\n"
    "v_lshlrev_b32_e32 v44, 16, v28\n"
    "v_fma_f32 v39, -v43, v45, 1.0\n"
    "v_mul_f32_e32 v46, 0xbfb8aa3b, v44\n"
    "v_fmac_f32_e32 v45, v39, v45\n"
    "v_div_scale_f32 v39, vcc, v27, v40, v27\n"
    "v_exp_f32_e32 v46, v46\n"
    "v_mul_f32_e32 v41, v39, v45\n"
    "v_fma_f32 v47, -v43, v41, v39\n"
    "v_fmac_f32_e32 v41, v47, v45\n"
    "v_fma_f32 v39, -v43, v41, v39\n"
    "v_add_f32_e32 v43, 1.0, v46\n"
    "v_div_scale_f32 v46, s[0:1], v43, v43, v44\n"
    "v_rcp_f32_e32 v47, v46\n"
    "v_div_fmas_f32 v39, v39, v45, v41\n"
    "v_and_b32_e32 v28, 0xffff0000, v28\n"
    "v_div_fixup_f32 v27, v39, v40, v27\n"
    "v_mul_f32_e32 v40, 0xbfb8aa3b, v28\n"
    "v_exp_f32_e32 v40, v40\n"
    "v_cvt_pk_bf16_f32 v39, v26, v27\n"
    "v_fma_f32 v26, -v46, v47, 1.0\n"
    "v_fmac_f32_e32 v47, v26, v47\n"
    "v_div_scale_f32 v26, vcc, v44, v43, v44\n"
    "v_mul_f32_e32 v27, v26, v47\n"
    "v_fma_f32 v41, -v46, v27, v26\n"
    "v_add_f32_e32 v40, 1.0, v40\n"
    "v_fmac_f32_e32 v27, v41, v47\n"
    "v_div_scale_f32 v41, s[0:1], v40, v40, v28\n"
    "v_rcp_f32_e32 v45, v41\n"
    "v_fma_f32 v26, -v46, v27, v26\n"
    "v_div_fmas_f32 v26, v26, v47, v27\n"
    "v_div_fixup_f32 v26, v26, v43, v44\n"
    "v_lshlrev_b32_e32 v44, 16, v29\n"
    "v_fma_f32 v27, -v41, v45, 1.0\n"
    "v_mul_f32_e32 v46, 0xbfb8aa3b, v44\n"
    "v_fmac_f32_e32 v45, v27, v45\n"
    "v_div_scale_f32 v27, vcc, v28, v40, v28\n"
    "v_exp_f32_e32 v46, v46\n"
    "v_mul_f32_e32 v43, v27, v45\n"
    "v_fma_f32 v47, -v41, v43, v27\n"
    "v_fmac_f32_e32 v43, v47, v45\n"
    "v_fma_f32 v27, -v41, v43, v27\n"
    "v_add_f32_e32 v41, 1.0, v46\n"
    "v_div_scale_f32 v46, s[0:1], v41, v41, v44\n"
    "v_rcp_f32_e32 v47, v46\n"
    "v_div_fmas_f32 v27, v27, v45, v43\n"
    "v_div_fixup_f32 v27, v27, v40, v28\n"
    "v_and_b32_e32 v28, 0xffff0000, v29\n"
    "v_mul_f32_e32 v29, 0xbfb8aa3b, v28\n"
    "v_exp_f32_e32 v29, v29\n"
    "v_cvt_pk_bf16_f32 v40, v26, v27\n"
    "v_fma_f32 v26, -v46, v47, 1.0\n"
    "v_fmac_f32_e32 v47, v26, v47\n"
    "v_div_scale_f32 v26, vcc, v44, v41, v44\n"
    "v_mul_f32_e32 v27, v26, v47\n"
    "v_fma_f32 v43, -v46, v27, v26\n"
    "v_add_f32_e32 v29, 1.0, v29\n"
    "v_fmac_f32_e32 v27, v43, v47\n"
    "v_div_scale_f32 v43, s[0:1], v29, v29, v28\n"
    "v_rcp_f32_e32 v45, v43\n"
    "v_fma_f32 v26, -v46, v27, v26\n"
    "v_div_fmas_f32 v26, v26, v47, v27\n"
    "v_div_fixup_f32 v26, v26, v41, v44\n"
    "v_fma_f32 v27, -v43, v45, 1.0\n"
    "v_fmac_f32_e32 v45, v27, v45\n"
    "v_div_scale_f32 v27, vcc, v28, v29, v28\n"
    "v_mul_f32_e32 v41, v27, v45\n"
    "v_fma_f32 v44, -v43, v41, v27\n"
    "v_fmac_f32_e32 v41, v44, v45\n"
    "v_fma_f32 v27, -v43, v41, v27\n"
    "v_div_fmas_f32 v27, v27, v45, v41\n"
    "v_div_fixup_f32 v27, v27, v29, v28\n"
    "v_cvt_pk_bf16_f32 v41, v26, v27\n"
    "v_perm_b32 v26, v34, v30, s2\n"
    "v_perm_b32 v27, v35, v31, s2\n"
    "v_perm_b32 v28, v36, v32, s2\n"
    "v_perm_b32 v29, v37, v33, s2\n"
    "v_perm_b32 v30, v34, v30, s3\n"
    "v_perm_b32 v31, v35, v31, s3\n"
    "v_perm_b32 v32, v36, v32, s3\n"
    "v_perm_b32 v33, v37, v33, s3\n"
    "v_perm_b32 v34, v22, v18, s2\n"
    "v_perm_b32 v35, v23, v19, s2\n"
    "v_perm_b32 v36, v24, v20, s2\n"
    "v_perm_b32 v37, v25, v21, s2\n"
    "v_perm_b32 v18, v22, v18, s3\n"
    "v_perm_b32 v19, v23, v19, s3\n"
    "v_perm_b32 v20, v24, v20, s3\n"
    "v_perm_b32 v21, v25, v21, s3\n"
    "v_perm_b32 v22, v14, v10, s2\n"
    "v_perm_b32 v23, v15, v11, s2\n"
    "v_perm_b32 v24, v16, v12, s2\n"
    "v_perm_b32 v25, v17, v13, s2\n"
    "v_perm_b32 v10, v14, v10, s3\n"
    "v_perm_b32 v11, v15, v11, s3\n"
    "v_perm_b32 v12, v16, v12, s3\n"
    "v_perm_b32 v13, v17, v13, s3\n"
    "v_perm_b32 v14, v6, v2, s2\n"
    "v_perm_b32 v15, v7, v3, s2\n"
    "v_perm_b32 v16, v8, v4, s2\n"
    "v_perm_b32 v17, v9, v5, s2\n"
    "v_perm_b32 v2, v6, v2, s3\n"
    "v_perm_b32 v3, v7, v3, s3\n"
    "v_perm_b32 v4, v8, v4, s3\n"
    "v_perm_b32 v5, v9, v5, s3\n"
    "v_mov_b32_e32 v6, 0\n"
    "v_mov_b32_e32 v7, 0\n"
    "v_mov_b32_e32 v8, 0\n"
    "v_mov_b32_e32 v9, 0\n"
    "v_mov_b32_e32 v43, 0\n"
    "v_mov_b32_e32 v44, 0\n"
    "v_mov_b32_e32 v45, 0\n"
    "s_nop 0\n"
    "v_dot2c_f32_bf16_e32 v6, v26, v38\n"
    "v_dot2c_f32_bf16_e32 v7, v30, v38\n"
    "v_dot2c_f32_bf16_e32 v8, v27, v38\n"
    "v_dot2c_f32_bf16_e32 v9, v31, v38\n"
    "v_dot2c_f32_bf16_e32 v43, v28, v38\n"
    "v_dot2c_f32_bf16_e32 v44, v32, v38\n"
    "v_dot2c_f32_bf16_e32 v45, v29, v38\n"
    "v_dot2c_f32_bf16_e32 v42, v33, v38\n"
    "v_dot2c_f32_bf16_e32 v6, v34, v39\n"
    "v_dot2c_f32_bf16_e32 v7, v18, v39\n"
    "v_dot2c_f32_bf16_e32 v8, v35, v39\n"
    "v_dot2c_f32_bf16_e32 v9, v19, v39\n"
    "v_dot2c_f32_bf16_e32 v43, v36, v39\n"
    "v_dot2c_f32_bf16_e32 v44, v20, v39\n"
    "v_dot2c_f32_bf16_e32 v45, v37, v39\n"
    "v_dot2c_f32_bf16_e32 v42, v21, v39\n"
    "v_dot2c_f32_bf16_e32 v6, v22, v40\n"
    "v_dot2c_f32_bf16_e32 v7, v10, v40\n"
    "v_dot2c_f32_bf16_e32 v8, v23, v40\n"
    "v_dot2c_f32_bf16_e32 v9, v11, v40\n"
    "v_dot2c_f32_bf16_e32 v43, v24, v40\n"
    "v_dot2c_f32_bf16_e32 v44, v12, v40\n"
    "v_dot2c_f32_bf16_e32 v45, v25, v40\n"
    "v_dot2c_f32_bf16_e32 v42, v13, v40\n"
    "v_dot2c_f32_bf16_e32 v6, v14, v41\n"
    "v_dot2c_f32_bf16_e32 v7, v2, v41\n"
    "v_dot2c_f32_bf16_e32 v8, v15, v41\n"
    "v_dot2c_f32_bf16_e32 v9, v3, v41\n"
    "v_dot2c_f32_bf16_e32 v43, v16, v41\n"
    "v_dot2c_f32_bf16_e32 v44, v4, v41\n"
    "v_dot2c_f32_bf16_e32 v45, v17, v41\n"
    "v_dot2c_f32_bf16_e32 v42, v5, v41\n"
    "v_permlane32_swap_b32_e32 v6, v43\n"
    "v_permlane32_swap_b32_e32 v7, v44\n"
    "v_permlane32_swap_b32_e32 v8, v45\n"
    "v_permlane32_swap_b32_e32 v9, v42\n"
    "v_add_f32_e32 v2, v6, v43\n"
    "v_add_f32_e32 v3, v7, v44\n"
    "v_add_f32_e32 v4, v8, v45\n"
    "v_add_f32_e32 v5, v9, v42\n"
    "s_nop 0\n"
    "v_permlane16_swap_b32_e32 v2, v4\n"
    "v_permlane16_swap_b32_e32 v3, v5\n"
    "v_add_f32_e32 v2, v2, v4\n"
    "v_add_f32_e32 v3, v3, v5\n"
    "v_lshlrev_b32_e32 v0, 2, v0\n"
    "v_add_f32_dpp v2, v2, v2 row_ror:8 row_mask:0xf bank_mask:0xf bound_ctrl:1\n"
    "v_add_f32_dpp v3, v3, v3 row_ror:8 row_mask:0xf bank_mask:0xf bound_ctrl:1\n"
    "s_nop 1\n"
    "v_mov_b32_dpp v2, v3 quad_perm:[0,1,2,3] row_mask:0xf bank_mask:0xc\n"
    "ds_write_b32 v0, v2 offset:2048\n"
    "s_waitcnt lgkmcnt(0)\n"
    "s_barrier\n"
    "s_endpgm\n"
    ".p2align 6\n"
    ".Lfb_role_a_tail:\n"
    "v_bfe_u32 v10, v0, 5, 1\n"
    "s_lshl_b32 s6, s9, 5\n"
    "v_lshlrev_b32_e32 v12, 2, v6\n"
    "v_bfe_u32 v13, v1, 3, 2\n"
    "s_load_dwordx2 s[4:5], s[0:1], 0x58\n"
    "v_and_b32_e32 v8, 3, v0\n"
    "v_and_b32_e32 v9, 48, v0\n"
    "s_movk_i32 s1, 0x110\n"
    "v_lshlrev_b32_e32 v11, 9, v10\n"
    "v_or3_b32 v12, v13, v12, s6\n"
    "v_and_b32_e32 v14, 31, v0\n"
    "v_mad_u32_u24 v9, v8, s1, v9\n"
    "s_lshl_b32 s1, s11, 10\n"
    "v_lshlrev_b32_e32 v12, 2, v12\n"
    "v_or_b32_e32 v13, s12, v11\n"
    "v_or_b32_e32 v15, s6, v14\n"
    "v_add3_u32 v12, v13, s1, v12\n"
    "v_lshlrev_b32_e32 v13, 12, v10\n"
    "v_lshlrev_b32_e32 v15, 4, v15\n"
    "v_add3_u32 v13, v13, v15, s13\n"
    "s_lshl_b32 s13, s9, 7\n"
    "v_add_u32_e32 v11, s13, v11\n"
    "v_lshlrev_b32_e32 v14, 1, v14\n"
    "v_lshlrev_b32_e32 v15, 4, v1\n"
    "v_mov_b32_e32 v17, 0x580\n"
    "s_add_i32 s1, s10, 0x1cc00\n"
    "v_or_b32_e32 v11, v11, v14\n"
    "v_lshl_or_b32 v16, s9, 11, v15\n"
    "v_lshl_or_b32 v8, v8, 4, v17\n"
    "v_lshl_or_b32 v15, s9, 13, v15\n"
    "v_lshl_add_u32 v11, v11, 3, s1\n"
    "s_mul_i32 s1, s9, 0x110\n"
    "v_or_b32_e32 v10, v14, v10\n"
    "s_waitcnt lgkmcnt(0)\n"
    "s_load_dword s0, s[4:5], 0x0\n"
    "v_add_u32_e32 v16, 0x1000, v16\n"
    "v_lshl_add_u32 v17, s9, 2, v8\n"
    "v_add_u32_e32 v30, 0x4000, v15\n"
    "v_lshl_add_u32 v10, v10, 1, s1\n"
    "s_movk_i32 s1, 0x200\n"
    "s_movk_i32 s14, 0x2200\n"
    "v_lshl_or_b32 v2, v2, 8, s17\n"
    "v_bfe_u32 v4, v0, 2, 2\n"
    "s_waitcnt lgkmcnt(0)\n"
    "s_waitcnt vmcnt(16)\n"
    "v_or_b32_e32 v14, v2, v5\n"
    "v_bitop3_b32 v15, v2, 64, v5 bitop3:0x36\n"
    "v_bitop3_b32 v18, v2, s18, v5 bitop3:0x36\n"
    "v_bitop3_b32 v2, v2, s19, v5 bitop3:0x36\n"
    "v_lshlrev_b32_e32 v5, 3, v0\n"
    "ds_read_b128 v[64:67], v14 offset:0\n"
    "ds_read_b128 v[68:71], v15 offset:0\n"
    "ds_read_b128 v[72:75], v18 offset:0\n"
    "ds_read_b128 v[76:79], v2 offset:0\n"
    "ds_read_b128 v[80:83], v14 offset:4096\n"
    "ds_read_b128 v[84:87], v15 offset:4096\n"
    "ds_read_b128 v[88:91], v18 offset:4096\n"
    "ds_read_b128 v[92:95], v2 offset:4096\n"
    "ds_read_b128 v[96:99], v14 offset:8192\n"
    "ds_read_b128 v[100:103], v15 offset:8192\n"
    "ds_read_b128 v[104:107], v18 offset:8192\n"
    "ds_read_b128 v[108:111], v2 offset:8192\n"
    "s_waitcnt lgkmcnt(4)\n"
    "ds_read_b128 v[112:115], v14 offset:12288\n"
    "ds_read_b128 v[116:119], v15 offset:12288\n"
    "ds_read_b128 v[120:123], v18 offset:12288\n"
    "ds_read_b128 v[124:127], v2 offset:12288\n"
    "s_waitcnt lgkmcnt(0)\n"
    "v_lshlrev_b32_e32 v2, 8, v4\n"
    "v_lshlrev_b32_e32 v4, 6, v4\n"
    "v_lshlrev_b32_e32 v3, 5, v3\n"
    "v_and_b32_e32 v14, 16, v5\n"
    "v_bitop3_b32 v3, v3, v4, v14 bitop3:0x36\n"
    "v_and_b32_e32 v4, 8, v5\n"
    "v_or3_b32 v2, v2, v4, v3\n"
    "v_mov_b32_e32 v4, 0x80\n"
    "v_or_b32_e32 v3, s21, v2\n"
    "v_bitop3_b32 v15, s21, v4, v2 bitop3:0x36\n"
    "v_mov_b32_e32 v4, 0x400\n"
    "v_xad_u32 v18, v3, 16, v4\n"
    "v_mov_b32_e32 v4, 0x90\n"
    "v_bitop3_b32 v4, s21, v4, v2 bitop3:0x36\n"
    "v_add_u32_e32 v19, 0x400, v4\n"
    "v_mov_b32_e32 v4, 0xa0\n"
    "v_bitop3_b32 v4, s21, v4, v2 bitop3:0x36\n"
    "v_mov_b32_e32 v20, 0x800\n"
    "v_add_u32_e32 v22, 0x800, v4\n"
    "v_mov_b32_e32 v4, 0xc00\n"
    "v_mov_b32_e32 v14, v3\n"
    "v_xad_u32 v21, v3, 32, v20\n"
    "v_xad_u32 v23, v3, 48, v4\n"
    "v_mov_b32_e32 v3, 0xb0\n"
    "v_bitop3_b32 v2, s21, v3, v2 bitop3:0x36\n"
    "v_add_u32_e32 v24, 0xc00, v2\n"
    "s_mov_b32 s6, 0x83400\n"
    "s_waitcnt vmcnt(12)\n"
    "ds_read_b64_tr_b16 v[128:129], v14 offset:0\n"
    "ds_read_b64_tr_b16 v[130:131], v15 offset:0\n"
    "ds_read_b64_tr_b16 v[132:133], v18 offset:0\n"
    "ds_read_b64_tr_b16 v[134:135], v19 offset:0\n"
    "ds_read_b64_tr_b16 v[136:137], v21 offset:0\n"
    "ds_read_b64_tr_b16 v[138:139], v22 offset:0\n"
    "ds_read_b64_tr_b16 v[140:141], v23 offset:0\n"
    "ds_read_b64_tr_b16 v[142:143], v24 offset:0\n"
    "s_waitcnt lgkmcnt(0)\n"
    "s_waitcnt vmcnt(8)\n"
    "ds_read_b64_tr_b16 v[144:145], v14 offset:4096\n"
    "ds_read_b64_tr_b16 v[146:147], v15 offset:4096\n"
    "ds_read_b64_tr_b16 v[148:149], v18 offset:4096\n"
    "ds_read_b64_tr_b16 v[150:151], v19 offset:4096\n"
    "ds_read_b64_tr_b16 v[152:153], v21 offset:4096\n"
    "ds_read_b64_tr_b16 v[154:155], v22 offset:4096\n"
    "ds_read_b64_tr_b16 v[156:157], v23 offset:4096\n"
    "ds_read_b64_tr_b16 v[158:159], v24 offset:4096\n"
    "s_waitcnt lgkmcnt(0)\n"
    "s_waitcnt vmcnt(4)\n"
    "s_waitcnt lgkmcnt(0)\n"
    "s_barrier\n"
    "s_and_b32 s5, s5, 0xffff\n"
    "buffer_load_dwordx4 v[224:227], v11, s[4:7], 0 offen sc1\n"
    "buffer_load_dwordx4 v[228:231], v11, s[4:7], s2 offen sc1\n"
    "buffer_load_dwordx4 v[232:235], v11, s[4:7], s1 offen sc1\n"
    "buffer_load_dwordx4 v[236:239], v11, s[4:7], s14 offen sc1\n"
    "s_waitcnt vmcnt(8)\n"
    "ds_read_b64_tr_b16 v[160:161], v14 offset:8192\n"
    "ds_read_b64_tr_b16 v[162:163], v15 offset:8192\n"
    "ds_read_b64_tr_b16 v[164:165], v18 offset:8192\n"
    "ds_read_b64_tr_b16 v[166:167], v19 offset:8192\n"
    "ds_read_b64_tr_b16 v[168:169], v21 offset:8192\n"
    "ds_read_b64_tr_b16 v[170:171], v22 offset:8192\n"
    "ds_read_b64_tr_b16 v[172:173], v23 offset:8192\n"
    "ds_read_b64_tr_b16 v[174:175], v24 offset:8192\n"
    "s_waitcnt lgkmcnt(0)\n"
    "s_add_i32 s0, s0, 1\n"
    "s_mov_b32 s15, 0x20000\n"
    "s_branch 3f\n"
    "1:\n"
    "s_sleep 2\n"
    "buffer_load_dwordx4 v[224:227], v11, s[4:7], 0 offen sc1\n"
    "buffer_load_dwordx4 v[228:231], v11, s[4:7], s2 offen sc1\n"
    "buffer_load_dwordx4 v[232:235], v11, s[4:7], s1 offen sc1\n"
    "buffer_load_dwordx4 v[236:239], v11, s[4:7], s14 offen sc1\n"
    "3:\n"
    "s_waitcnt vmcnt(0)\n"
    "v_xor_b32 v2, s0, v225\n"
    "v_bitop3_b32 v2, v227, v2, s0 bitop3:0xde\n"
    "v_bitop3_b32 v2, v229, v2, s0 bitop3:0xde\n"
    "v_bitop3_b32 v2, v231, v2, s0 bitop3:0xde\n"
    "v_bitop3_b32 v2, v233, v2, s0 bitop3:0xde\n"
    "v_bitop3_b32 v2, v235, v2, s0 bitop3:0xde\n"
    "v_bitop3_b32 v2, v237, v2, s0 bitop3:0xde\n"
    "v_bitop3_b32 v2, v239, v2, s0 bitop3:0xde\n"
    "v_cmp_ne_u32 vcc, 0, v2\n"
    "s_cbranch_vccz 2f\n"
    "s_mov_b64 exec, vcc\n"
    "s_sub_u32 s15, s15, 1\n"
    "s_cbranch_scc0 1b\n"
    "2:\n"
    "s_mov_b64 exec, -1\n"
    "s_lshl_b32 s0, s11, 6\n"
    "v_add_f32_e32 v2, v224, v228\n"
    "v_add_f32_e32 v4, v226, v230\n"
    "v_add_f32_e32 v3, v232, v236\n"
    "v_add_f32_e32 v5, v234, v238\n"
    "v_permlane32_swap_b32_e32 v2, v4\n"
    "s_nop 0\n"
    "v_permlane32_swap_b32_e32 v3, v5\n"
    "v_pk_add_f32 v[2:3], v[2:3], v[4:5]\n"
    "s_and_b32 s0, s0, 0xfffffe00\n"
    "v_cvt_pk_bf16_f32 v2, v2, v3\n"
    "ds_write_b16 v10, v2\n"
    "ds_write_b16_d16_hi v10, v2 offset:128\n"
    "s_waitcnt lgkmcnt(0)\n"
    "s_barrier\n"
    "v_lshlrev_b32_e32 v2, 4, v6\n"
    "s_add_i32 s1, s13, s0\n"
    "ds_read_b128 v[192:195], v9 offset:0\n"
    "ds_read_b128 v[196:199], v9 offset:64\n"
    "ds_read_b128 v[200:203], v9 offset:128\n"
    "ds_read_b128 v[204:207], v9 offset:192\n"
    "s_waitcnt lgkmcnt(3)\n"
    "v_mfma_f32_16x16x32_bf16 v[208:211], v[64:67], v[192:195], 0\n"
    "ds_read_b64_tr_b16 v[176:177], v14 offset:12288\n"
    "v_mfma_f32_16x16x32_bf16 v[212:215], v[80:83], v[192:195], 0\n"
    "ds_read_b64_tr_b16 v[178:179], v15 offset:12288\n"
    "s_waitcnt lgkmcnt(4)\n"
    "v_mfma_f32_16x16x32_bf16 v[208:211], v[68:71], v[196:199], v[208:211]\n"
    "ds_read_b64_tr_b16 v[180:181], v18 offset:12288\n"
    "v_mfma_f32_16x16x32_bf16 v[212:215], v[84:87], v[196:199], v[212:215]\n"
    "ds_read_b64_tr_b16 v[182:183], v19 offset:12288\n"
    "s_waitcnt lgkmcnt(5)\n"
    "v_mfma_f32_16x16x32_bf16 v[208:211], v[72:75], v[200:203], v[208:211]\n"
    "ds_read_b64_tr_b16 v[184:185], v21 offset:12288\n"
    "v_mfma_f32_16x16x32_bf16 v[212:215], v[88:91], v[200:203], v[212:215]\n"
    "ds_read_b64_tr_b16 v[186:187], v22 offset:12288\n"
    "s_waitcnt lgkmcnt(6)\n"
    "v_mfma_f32_16x16x32_bf16 v[208:211], v[76:79], v[204:207], v[208:211]\n"
    "ds_read_b64_tr_b16 v[188:189], v23 offset:12288\n"
    "v_mfma_f32_16x16x32_bf16 v[212:215], v[92:95], v[204:207], v[212:215]\n"
    "ds_read_b64_tr_b16 v[190:191], v24 offset:12288\n"
    "v_mfma_f32_16x16x32_bf16 v[216:219], v[96:99], v[192:195], 0\n"
    "v_mfma_f32_16x16x32_bf16 v[220:223], v[112:115], v[192:195], 0\n"
    "v_mfma_f32_16x16x32_bf16 v[216:219], v[100:103], v[196:199], v[216:219]\n"
    "v_mfma_f32_16x16x32_bf16 v[220:223], v[116:119], v[196:199], v[220:223]\n"
    "v_mfma_f32_16x16x32_bf16 v[216:219], v[104:107], v[200:203], v[216:219]\n"
    "s_nop 1\n"
    "v_mfma_f32_16x16x32_bf16 v[220:223], v[120:123], v[200:203], v[220:223]\n"
    "v_mov_b32_dpp v208, v212 quad_perm:[0,1,2,3] row_mask:0xf bank_mask:0x2\n"
    "v_mov_b32_dpp v209, v213 quad_perm:[0,1,2,3] row_mask:0xf bank_mask:0x2\n"
    "v_mfma_f32_16x16x32_bf16 v[216:219], v[108:111], v[204:207], v[216:219]\n"
    "v_mov_b32_dpp v210, v214 quad_perm:[0,1,2,3] row_mask:0xf bank_mask:0x2\n"
    "v_mov_b32_dpp v211, v215 quad_perm:[0,1,2,3] row_mask:0xf bank_mask:0x2\n"
    "v_mfma_f32_16x16x32_bf16 v[220:223], v[124:127], v[204:207], v[220:223]\n"
    "s_nop 4\n"
    "v_mov_b32_dpp v208, v216 quad_perm:[0,1,2,3] row_mask:0xf bank_mask:0x4\n"
    "v_mov_b32_dpp v209, v217 quad_perm:[0,1,2,3] row_mask:0xf bank_mask:0x4\n"
    "v_mov_b32_dpp v210, v218 quad_perm:[0,1,2,3] row_mask:0xf bank_mask:0x4\n"
    "v_mov_b32_dpp v211, v219 quad_perm:[0,1,2,3] row_mask:0xf bank_mask:0x4\n"
    "v_mov_b32_dpp v208, v220 quad_perm:[0,1,2,3] row_mask:0xf bank_mask:0x8\n"
    "v_mov_b32_dpp v209, v221 quad_perm:[0,1,2,3] row_mask:0xf bank_mask:0x8\n"
    "v_mov_b32_dpp v210, v222 quad_perm:[0,1,2,3] row_mask:0xf bank_mask:0x8\n"
    "v_mov_b32_dpp v211, v223 quad_perm:[0,1,2,3] row_mask:0xf bank_mask:0x8\n"
    "v_max3_f32 v40, v208, v209, v210\n"
    "v_cvt_pk_bf16_f32 v36, 0, v208\n"
    "v_max_f32 v40, v40, v211\n"
    "v_cvt_pk_bf16_f32 v37, 0, v209\n"
    "v_cvt_pk_bf16_f32 v38, 0, v210\n"
    "v_max_f32_dpp v40, v40, v40 row_ror:4 row_mask:0xf bank_mask:0xf\n"
    "v_cvt_pk_bf16_f32 v39, 0, v211\n"
    "v_mul_f32 v36, 0x3db504f3, v36\n"
    "v_max_f32_dpp v40, v40, v40 row_ror:8 row_mask:0xf bank_mask:0xf\n"
    "v_mul_f32 v37, 0x3db504f3, v37\n"
    "v_mov_b32 v41, v40\n"
    "v_mul_f32 v38, 0x3db504f3, v38\n"
    "v_mul_f32 v39, 0x3db504f3, v39\n"
    "v_permlane16_swap_b32 v40, v41\n"
    "v_max_f32 v40, v40, v41\n"
    "v_cvt_pk_bf16_f32 v36, 0, v36\n"
    "v_mov_b32 v41, v40\n"
    "v_cvt_pk_bf16_f32 v37, 0, v37\n"
    "v_cvt_pk_bf16_f32 v38, 0, v38\n"
    "v_permlane32_swap_b32 v40, v41\n"
    "v_max_f32 v40, v40, v41\n"
    "ds_write_b32 v17, v40\n"
    "v_cvt_pk_bf16_f32 v39, 0, v39\n"
    "v_mul_f32 v36, 0x3fb8aa3b, v36\n"
    "v_mul_f32 v37, 0x3fb8aa3b, v37\n"
    "v_mul_f32 v38, 0x3fb8aa3b, v38\n"
    "v_mul_f32 v39, 0x3fb8aa3b, v39\n"
    "s_waitcnt lgkmcnt(0)\n"
    "s_barrier\n"
    "ds_read_b128 v[52:55], v8\n"
    "s_waitcnt lgkmcnt(0)\n"
    "v_max3_f32 v40, v52, v53, v54\n"
    "v_max_f32 v40, v40, v55\n"
    "v_cvt_pk_bf16_f32 v40, 0, v40\n"
    "v_mul_f32 v40, 0x3db504f3, v40\n"
    "v_cvt_pk_bf16_f32 v40, 0, v40\n"
    "v_mul_f32 v40, 0x3fb8aa3b, v40\n"
    "v_sub_f32 v36, v36, v40\n"
    "v_sub_f32 v37, v37, v40\n"
    "v_exp_f32 v36, v36\n"
    "v_exp_f32 v37, v37\n"
    "v_sub_f32 v38, v38, v40\n"
    "v_sub_f32 v39, v39, v40\n"
    "v_exp_f32 v38, v38\n"
    "v_exp_f32 v39, v39\n"
    "v_cvt_pk_bf16_f32 v42, v36, v37\n"
    "v_cvt_pk_bf16_f32 v43, v38, v39\n"
    "v_add_f32 v36, v36, v37\n"
    "v_add_f32 v38, v38, v39\n"
    "v_mfma_f32_4x4x4_16b_bf16 v[44:47], v[42:43], v[128:129], 0 cbsz:4 abid:0\n"
    "v_mfma_f32_4x4x4_16b_bf16 v[48:51], v[42:43], v[130:131], 0 cbsz:4 abid:0\n"
    "v_add_f32 v36, v36, v38\n"
    "v_mfma_f32_4x4x4_16b_bf16 v[44:47], v[42:43], v[132:133], v[44:47] cbsz:4 abid:4\n"
    "v_mfma_f32_4x4x4_16b_bf16 v[48:51], v[42:43], v[134:135], v[48:51] cbsz:4 abid:4\n"
    "s_nop 0\n"
    "v_mfma_f32_4x4x4_16b_bf16 v[44:47], v[42:43], v[136:137], v[44:47] cbsz:4 abid:8\n"
    "v_mfma_f32_4x4x4_16b_bf16 v[48:51], v[42:43], v[138:139], v[48:51] cbsz:4 abid:8\n"
    "v_add_f32_dpp v36, v36, v36 row_ror:4 row_mask:0xf bank_mask:0xf\n"
    "v_mfma_f32_4x4x4_16b_bf16 v[44:47], v[42:43], v[140:141], v[44:47] cbsz:4 abid:12\n"
    "v_mfma_f32_4x4x4_16b_bf16 v[48:51], v[42:43], v[142:143], v[48:51] cbsz:4 abid:12\n"
    "s_nop 0\n"
    "v_mfma_f32_4x4x4_16b_bf16 v[44:47], v[42:43], v[144:145], v[44:47] cbsz:4 abid:1\n"
    "v_mfma_f32_4x4x4_16b_bf16 v[48:51], v[42:43], v[146:147], v[48:51] cbsz:4 abid:1\n"
    "v_add_f32_dpp v36, v36, v36 row_ror:8 row_mask:0xf bank_mask:0xf\n"
    "v_mfma_f32_4x4x4_16b_bf16 v[44:47], v[42:43], v[148:149], v[44:47] cbsz:4 abid:5\n"
    "v_mfma_f32_4x4x4_16b_bf16 v[48:51], v[42:43], v[150:151], v[48:51] cbsz:4 abid:5\n"
    "v_mov_b32 v37, v36\n"
    "v_mfma_f32_4x4x4_16b_bf16 v[44:47], v[42:43], v[152:153], v[44:47] cbsz:4 abid:9\n"
    "v_mfma_f32_4x4x4_16b_bf16 v[48:51], v[42:43], v[154:155], v[48:51] cbsz:4 abid:9\n"
    "v_permlane16_swap_b32 v36, v37\n"
    "v_mfma_f32_4x4x4_16b_bf16 v[44:47], v[42:43], v[156:157], v[44:47] cbsz:4 abid:13\n"
    "v_mfma_f32_4x4x4_16b_bf16 v[48:51], v[42:43], v[158:159], v[48:51] cbsz:4 abid:13\n"
    "v_add_f32 v36, v36, v37\n"
    "v_mfma_f32_4x4x4_16b_bf16 v[44:47], v[42:43], v[160:161], v[44:47] cbsz:4 abid:2\n"
    "v_mfma_f32_4x4x4_16b_bf16 v[48:51], v[42:43], v[162:163], v[48:51] cbsz:4 abid:2\n"
    "v_mov_b32 v37, v36\n"
    "v_mfma_f32_4x4x4_16b_bf16 v[44:47], v[42:43], v[164:165], v[44:47] cbsz:4 abid:6\n"
    "v_mfma_f32_4x4x4_16b_bf16 v[48:51], v[42:43], v[166:167], v[48:51] cbsz:4 abid:6\n"
    "v_permlane32_swap_b32 v36, v37\n"
    "v_mfma_f32_4x4x4_16b_bf16 v[44:47], v[42:43], v[168:169], v[44:47] cbsz:4 abid:10\n"
    "v_mfma_f32_4x4x4_16b_bf16 v[48:51], v[42:43], v[170:171], v[48:51] cbsz:4 abid:10\n"
    "v_add_f32 v36, v36, v37\n"
    "v_mfma_f32_4x4x4_16b_bf16 v[44:47], v[42:43], v[172:173], v[44:47] cbsz:4 abid:14\n"
    "v_mfma_f32_4x4x4_16b_bf16 v[48:51], v[42:43], v[174:175], v[48:51] cbsz:4 abid:14\n"
    "ds_write_b32 v17, v36 offset:64\n"
    "v_mfma_f32_4x4x4_16b_bf16 v[44:47], v[42:43], v[176:177], v[44:47] cbsz:4 abid:3\n"
    "v_mfma_f32_4x4x4_16b_bf16 v[48:51], v[42:43], v[178:179], v[48:51] cbsz:4 abid:3\n"
    "s_nop 0\n"
    "v_mfma_f32_4x4x4_16b_bf16 v[44:47], v[42:43], v[180:181], v[44:47] cbsz:4 abid:7\n"
    "s_nop 1\n"
    "v_mfma_f32_4x4x4_16b_bf16 v[44:47], v[42:43], v[184:185], v[44:47] cbsz:4 abid:11\n"
    "v_mfma_f32_4x4x4_16b_bf16 v[48:51], v[42:43], v[182:183], v[48:51] cbsz:4 abid:7\n"
    "s_nop 0\n"
    "v_mfma_f32_4x4x4_16b_bf16 v[44:47], v[42:43], v[188:189], v[44:47] cbsz:4 abid:15\n"
    "v_mfma_f32_4x4x4_16b_bf16 v[48:51], v[42:43], v[186:187], v[48:51] cbsz:4 abid:11\n"
    "s_nop 1\n"
    "v_mfma_f32_4x4x4_16b_bf16 v[48:51], v[42:43], v[190:191], v[48:51] cbsz:4 abid:15\n"
    "s_nop 0\n"
    "ds_write_b128 v16, v[44:47]\n"
    "s_nop 2\n"
    "ds_write_b128 v16, v[48:51] offset:1024\n"
    "s_waitcnt lgkmcnt(0)\n"
    "s_barrier\n"
    "ds_read_b128 v[44:47], v13\n"
    "ds_read_b128 v[48:51], v13 offset:2048\n"
    "s_waitcnt lgkmcnt(0)\n"
    "v_pk_add_f32 v[44:45], v[44:45], v[48:49]\n"
    "v_pk_add_f32 v[46:47], v[46:47], v[50:51]\n"
    "s_nop 1\n"
    "v_permlane32_swap_b32 v44, v46\n"
    "v_permlane32_swap_b32 v45, v47\n"
    "v_add_f32 v44, v44, v46\n"
    "v_add_f32 v45, v45, v47\n"
    "v_cvt_pk_bf16_f32 v44, v44, v45\n"
    "v_min_u32 v44, -2, v44\n"
    "buffer_store_dword v44, v12, s[4:7], 0 offen sc1\n"
    "v_lshl_or_b32 v2, v7, 10, v2\n"
    "s_add_i32 s1, s1, s12\n"
    "v_add_u32_e32 v38, s1, v2\n"
    "v_lshlrev_b32_e32 v2, 2, v7\n"
    "v_lshl_or_b32 v2, v6, 5, v2\n"
    "v_lshl_or_b32 v2, s9, 8, v2\n"
    "s_lshl_b32 s1, s8, 6\n"
    "v_add_u32_e32 v62, 0x800, v2\n"
    "v_mov_b32_e32 v2, 0x600\n"
    "s_and_b32 s1, s1, 0x1c0\n"
    "v_and_or_b32 v63, v0, 56, v2\n"
    "v_or_b32_e32 v0, s1, v1\n"
    "s_add_i32 s10, s10, 0x1bc00\n"
    "v_or_b32_e32 v0, s0, v0\n"
    "v_mov_b32_e32 v54, 0\n"
    "v_mov_b32_e32 v55, 0\n"
    "v_mov_b32_e32 v56, 0\n"
    "v_mov_b32_e32 v57, 0\n"
    "v_mov_b32_e32 v58, 0\n"
    "v_mov_b32_e32 v59, 0\n"
    "v_mov_b32_e32 v60, 0\n"
    "v_mov_b32_e32 v61, 0\n"
    "v_lshl_add_u32 v0, v0, 2, s10\n"
    "v_lshl_or_b32 v1, v1, 2, v20\n"
    "s_waitcnt lgkmcnt(0)\n"
    "s_barrier\n"
    "buffer_load_dwordx4 v[224:227], v38, s[4:7], 0 offen sc1\n"
    "buffer_load_dwordx4 v[228:231], v38, s[4:7], s2 offen sc1\n"
    "ds_read_b128 v[2:5], v30\n"
    "ds_read_b128 v[6:9], v30 offset:1024\n"
    "ds_read_b128 v[10:13], v30 offset:2048\n"
    "ds_read_b128 v[14:17], v30 offset:3072\n"
    "ds_read_b128 v[18:21], v30 offset:4096\n"
    "ds_read_b128 v[22:25], v30 offset:5120\n"
    "ds_read_b128 v[26:29], v30 offset:6144\n"
    "ds_read_b128 v[30:33], v30 offset:7168\n"
    "s_mov_b32 s0, 0x5040100\n"
    "s_mov_b32 s1, 0x7060302\n"
    "s_waitcnt lgkmcnt(6)\n"
    "v_perm_b32 v34, v6, v2, s0\n"
    "v_perm_b32 v35, v7, v3, s0\n"
    "v_perm_b32 v36, v8, v4, s0\n"
    "v_perm_b32 v37, v9, v5, s0\n"
    "v_perm_b32 v2, v6, v2, s1\n"
    "v_perm_b32 v3, v7, v3, s1\n"
    "v_perm_b32 v4, v8, v4, s1\n"
    "v_perm_b32 v5, v9, v5, s1\n"
    "s_waitcnt lgkmcnt(4)\n"
    "v_perm_b32 v6, v14, v10, s0\n"
    "v_perm_b32 v7, v15, v11, s0\n"
    "v_perm_b32 v8, v16, v12, s0\n"
    "v_perm_b32 v9, v17, v13, s0\n"
    "v_perm_b32 v10, v14, v10, s1\n"
    "v_perm_b32 v11, v15, v11, s1\n"
    "v_perm_b32 v12, v16, v12, s1\n"
    "v_perm_b32 v13, v17, v13, s1\n"
    "s_waitcnt lgkmcnt(2)\n"
    "v_perm_b32 v14, v22, v18, s0\n"
    "v_perm_b32 v15, v23, v19, s0\n"
    "v_perm_b32 v16, v24, v20, s0\n"
    "v_perm_b32 v17, v25, v21, s0\n"
    "v_perm_b32 v18, v22, v18, s1\n"
    "v_perm_b32 v19, v23, v19, s1\n"
    "v_perm_b32 v20, v24, v20, s1\n"
    "v_perm_b32 v21, v25, v21, s1\n"
    "s_waitcnt lgkmcnt(0)\n"
    "v_perm_b32 v22, v30, v26, s0\n"
    "v_perm_b32 v23, v31, v27, s0\n"
    "v_perm_b32 v24, v32, v28, s0\n"
    "v_perm_b32 v25, v33, v29, s0\n"
    "v_perm_b32 v26, v30, v26, s1\n"
    "v_perm_b32 v27, v31, v27, s1\n"
    "v_perm_b32 v28, v32, v28, s1\n"
    "v_perm_b32 v29, v33, v29, s1\n"
    "s_mov_b32 s0, 0x20000\n"
    "s_branch 3f\n"
    "1:\n"
    "buffer_load_dwordx4 v[224:227], v38, s[4:7], 0 offen sc1\n"
    "buffer_load_dwordx4 v[228:231], v38, s[4:7], s2 offen sc1\n"
    "3:\n"
    "s_waitcnt vmcnt(0)\n"
    "v_max3_u32 v30, v224, v225, v226\n"
    "v_max3_u32 v30, v30, v227, v228\n"
    "v_max3_u32 v30, v30, v229, v230\n"
    "v_max3_u32 v30, v30, v231, v231\n"
    "v_cmp_eq_u32 vcc, -1, v30\n"
    "s_cbranch_vccz 2f\n"
    "s_mov_b64 exec, vcc\n"
    "s_sub_u32 s0, s0, 1\n"
    "s_cbranch_scc0 1b\n"
    "2:\n"
    "s_mov_b64 exec, -1\n"
    "s_cmp_gt_u32 s3, 63\n"
    "v_lshlrev_b32_e32 v38, 16, v224\n"
    "v_and_b32_e32 v39, 0xffff0000, v224\n"
    "v_lshlrev_b32_e32 v40, 16, v225\n"
    "v_and_b32_e32 v41, 0xffff0000, v225\n"
    "v_lshlrev_b32_e32 v42, 16, v226\n"
    "v_and_b32_e32 v43, 0xffff0000, v226\n"
    "v_lshlrev_b32_e32 v44, 16, v227\n"
    "v_and_b32_e32 v45, 0xffff0000, v227\n"
    "v_lshlrev_b32_e32 v46, 16, v228\n"
    "v_and_b32_e32 v47, 0xffff0000, v228\n"
    "v_lshlrev_b32_e32 v48, 16, v229\n"
    "v_and_b32_e32 v49, 0xffff0000, v229\n"
    "v_lshlrev_b32_e32 v50, 16, v230\n"
    "v_and_b32_e32 v51, 0xffff0000, v230\n"
    "v_lshlrev_b32_e32 v52, 16, v231\n"
    "v_and_b32_e32 v53, 0xffff0000, v231\n"
    "s_waitcnt lgkmcnt(0)\n"
    "s_barrier\n"
    "ds_read2_b64 v[30:33], v63 offset1:8\n"
    "s_waitcnt lgkmcnt(0)\n"
    "v_pk_mul_f32 v[46:47], v[46:47], v[32:33]\n"
    "s_nop 0\n"
    "v_pk_fma_f32 v[38:39], v[38:39], v[30:31], v[46:47]\n"
    "v_pk_mul_f32 v[46:47], v[48:49], v[32:33]\n"
    "s_nop 0\n"
    "v_pk_fma_f32 v[40:41], v[40:41], v[30:31], v[46:47]\n"
    "v_pk_mul_f32 v[46:47], v[50:51], v[32:33]\n"
    "v_pk_mul_f32 v[32:33], v[52:53], v[32:33]\n"
    "v_pk_fma_f32 v[42:43], v[42:43], v[30:31], v[46:47]\n"
    "v_pk_fma_f32 v[30:31], v[44:45], v[30:31], v[32:33]\n"
    "s_nop 0\n"
    "v_permlane32_swap_b32_e32 v38, v42\n"
    "v_permlane32_swap_b32_e32 v39, v43\n"
    "v_permlane32_swap_b32_e32 v40, v30\n"
    "v_permlane32_swap_b32_e32 v41, v31\n"
    "v_add_f32_e32 v32, v38, v42\n"
    "v_add_f32_e32 v33, v39, v43\n"
    "v_add_f32_e32 v30, v40, v30\n"
    "v_add_f32_e32 v31, v41, v31\n"
    "s_nop 0\n"
    "v_permlane16_swap_b32_e32 v32, v30\n"
    "v_permlane16_swap_b32_e32 v33, v31\n"
    "v_add_f32_e32 v30, v32, v30\n"
    "v_add_f32_e32 v31, v33, v31\n"
    "s_nop 0\n"
    "v_add_f32_dpp v30, v30, v30 row_ror:8 row_mask:0xf bank_mask:0xf bound_ctrl:1\n"
    "v_add_f32_dpp v31, v31, v31 row_ror:8 row_mask:0xf bank_mask:0xf bound_ctrl:1\n"
    "s_nop 1\n"
    "v_mov_b32_dpp v30, v31 quad_perm:[0,1,2,3] row_mask:0xf bank_mask:0xc\n"
    "s_nop 1\n"
    "v_mov_b32_dpp v31, v30 quad_perm:[1,0,3,2] row_mask:0xf bank_mask:0xf bound_ctrl:1\n"
    "v_cvt_pk_bf16_f32 v30, v30, v31\n"
    "v_mov_b32_e32 v31, v30\n"
    "s_nop 0\n"
    "v_mov_b32_dpp v30, v30 row_shl:4 row_mask:0xf bank_mask:0x5\n"
    "v_mov_b32_dpp v31, v31 row_shr:4 row_mask:0xf bank_mask:0xa\n"
    "s_nop 0\n"
    "v_mov_b32_dpp v33, v30 quad_perm:[0,0,0,0] row_mask:0xf bank_mask:0xf bound_ctrl:1\n"
    "v_mov_b32_dpp v32, v31 quad_perm:[0,0,0,0] row_mask:0xf bank_mask:0xf bound_ctrl:1\n"
    "v_mov_b32_dpp v31, v31 quad_perm:[2,2,2,2] row_mask:0xf bank_mask:0xf bound_ctrl:1\n"
    "v_dot2c_f32_bf16_e32 v55, v34, v32\n"
    "v_dot2c_f32_bf16_e32 v56, v2, v32\n"
    "v_dot2c_f32_bf16_e32 v57, v35, v32\n"
    "v_dot2c_f32_bf16_e32 v58, v3, v32\n"
    "v_dot2c_f32_bf16_e32 v59, v36, v32\n"
    "v_dot2c_f32_bf16_e32 v60, v4, v32\n"
    "v_dot2c_f32_bf16_e32 v61, v37, v32\n"
    "v_dot2c_f32_bf16_e32 v54, v5, v32\n"
    "v_dot2c_f32_bf16_e32 v55, v6, v31\n"
    "v_dot2c_f32_bf16_e32 v56, v10, v31\n"
    "v_dot2c_f32_bf16_e32 v57, v7, v31\n"
    "v_dot2c_f32_bf16_e32 v58, v11, v31\n"
    "v_dot2c_f32_bf16_e32 v59, v8, v31\n"
    "v_dot2c_f32_bf16_e32 v60, v12, v31\n"
    "v_dot2c_f32_bf16_e32 v61, v9, v31\n"
    "v_dot2c_f32_bf16_e32 v54, v13, v31\n"
    "v_mov_b32_dpp v30, v30 quad_perm:[2,2,2,2] row_mask:0xf bank_mask:0xf bound_ctrl:1\n"
    "v_dot2c_f32_bf16_e32 v55, v14, v33\n"
    "v_dot2c_f32_bf16_e32 v56, v18, v33\n"
    "v_dot2c_f32_bf16_e32 v57, v15, v33\n"
    "v_dot2c_f32_bf16_e32 v58, v19, v33\n"
    "v_dot2c_f32_bf16_e32 v59, v16, v33\n"
    "v_dot2c_f32_bf16_e32 v60, v20, v33\n"
    "v_dot2c_f32_bf16_e32 v61, v17, v33\n"
    "v_dot2c_f32_bf16_e32 v54, v21, v33\n"
    "v_dot2c_f32_bf16_e32 v55, v22, v30\n"
    "v_dot2c_f32_bf16_e32 v56, v26, v30\n"
    "v_dot2c_f32_bf16_e32 v57, v23, v30\n"
    "v_dot2c_f32_bf16_e32 v58, v27, v30\n"
    "v_dot2c_f32_bf16_e32 v59, v24, v30\n"
    "v_dot2c_f32_bf16_e32 v60, v28, v30\n"
    "v_dot2c_f32_bf16_e32 v61, v25, v30\n"
    "v_dot2c_f32_bf16_e32 v54, v29, v30\n"
    "v_permlane32_swap_b32_e32 v55, v59\n"
    "v_permlane32_swap_b32_e32 v56, v60\n"
    "v_permlane32_swap_b32_e32 v57, v61\n"
    "v_permlane32_swap_b32_e32 v58, v54\n"
    "v_add_f32_e32 v2, v55, v59\n"
    "v_add_f32_e32 v3, v56, v60\n"
    "v_add_f32_e32 v4, v57, v61\n"
    "v_add_f32_e32 v5, v58, v54\n"
    "s_nop 0\n"
    "v_permlane16_swap_b32_e32 v2, v4\n"
    "v_permlane16_swap_b32_e32 v3, v5\n"
    "v_add_f32_e32 v2, v2, v4\n"
    "v_add_f32_e32 v3, v3, v5\n"
    "s_nop 0\n"
    "v_add_f32_dpp v2, v2, v2 row_ror:8 row_mask:0xf bank_mask:0xf bound_ctrl:1\n"
    "v_add_f32_dpp v3, v3, v3 row_ror:8 row_mask:0xf bank_mask:0xf bound_ctrl:1\n"
    "s_nop 1\n"
    "v_mov_b32_dpp v2, v3 quad_perm:[0,1,2,3] row_mask:0xf bank_mask:0xc\n"
    "ds_write_b32 v62, v2\n"
    "s_waitcnt lgkmcnt(0)\n"
    "s_barrier\n"
    "s_cbranch_scc1 .Lfb_role_a_2\n"
    "ds_read2st64_b32 v[2:3], v1 offset1:1\n"
    "ds_read2st64_b32 v[4:5], v1 offset0:2 offset1:3\n"
    "s_waitcnt lgkmcnt(1)\n"
    "v_mov_b32_e32 v6, v2\n"
    "s_waitcnt lgkmcnt(0)\n"
    "v_mov_b32_e32 v7, v4\n"
    "v_mov_b32_e32 v4, v3\n"
    "v_pk_add_f32 v[2:3], v[6:7], v[4:5]\n"
    "s_nop 0\n"
    "v_pk_add_f32 v[2:3], v[2:3], v[2:3] op_sel:[0,1] op_sel_hi:[1,0]\n"
    "s_nop 0\n"
    "v_min_u32_e32 v1, -2, v2\n"
    "buffer_store_dword v1, v0, s[4:7], 0 offen sc1\n"
    ".Lfb_role_a_2:\n"
    "s_endpgm\n"
    ".p2align 6\n"
    ".Lfb_role_ah:\n"
    "s_and_b32 s13, s2, 7\n"
    "s_ashr_i32 s12, s2, 3\n"
    "s_mov_b64 s[2:3], s[92:93]\n"
    "s_load_dwordx2 s[4:5], s[0:1], 0x58\n"
    "v_and_b32_e32 v2, 63, v0\n"
    "s_mul_i32 s18, s13, 0x9800\n"
    "v_lshlrev_b32_e32 v1, 4, v2\n"
    "s_add_i32 s17, s12, -16\n"
    "s_waitcnt lgkmcnt(0)\n"
    "s_load_dword s15, s[4:5], 0x0\n"
    "v_or_b32_e32 v5, s18, v1\n"
    "v_lshl_add_u32 v3, s17, 10, v5\n"
    "v_mov_b32_e32 v6, -1\n"
    "s_ashr_i32 s16, s17, 3\n"
    "s_and_b32 s14, s12, 7\n"
    "s_mov_b32 s11, 0x20000\n"
    "s_mov_b32 s10, 0x83400\n"
    "s_and_b32 s9, s5, 0xffff\n"
    "s_mov_b32 s8, s4\n"
    "v_add_u32_e32 v3, 0x17000, v3\n"
    "v_mov_b32_e32 v7, v6\n"
    "v_mov_b32_e32 v8, v6\n"
    "v_mov_b32_e32 v9, v6\n"
    "v_cmp_lt_u32_e32 vcc, 1, v2\n"
    "buffer_store_dwordx4 v[6:9], v3, s[8:11], 0 offen offset:1024 sc1\n"
    "s_and_saveexec_b64 s[6:7], vcc\n"
    "s_xor_b64 s[6:7], exec, s[6:7]\n"
    "s_cbranch_execz .Lfb_role_ah_2\n"
    "s_lshl_b32 s5, s16, 11\n"
    "s_lshl_b32 s8, s14, 8\n"
    "s_or_b32 s5, s5, s8\n"
    "v_add_u32_e32 v3, s5, v5\n"
    "v_add_u32_e32 v3, 0x1bbe0, v3\n"
    "v_mov_b32_e32 v4, 0x7ffffff0\n"
    "v_cmp_gt_u32_e32 vcc, 18, v2\n"
    "s_lshl_b32 s5, s17, 7\n"
    "s_nop 0\n"
    "v_cndmask_b32_e32 v4, v4, v3, vcc\n"
    "v_mov_b32_e32 v3, s5\n"
    ".Lfb_role_ah_2:\n"
    "s_andn2_saveexec_b64 s[6:7], s[6:7]\n"
    "s_lshl_b32 s5, s17, 7\n"
    "v_add_u32_e32 v3, s5, v5\n"
    "v_add_u32_e32 v4, 0x1b400, v3\n"
    "v_mov_b32_e32 v3, s5\n"
    "s_or_b64 exec, exec, s[6:7]\n"
    "v_mov_b32_e32 v6, -1\n"
    "v_mov_b32_e32 v7, v6\n"
    "v_mov_b32_e32 v8, v6\n"
    "v_mov_b32_e32 v9, v6\n"
    "s_mov_b32 s5, s9\n"
    "s_mov_b32 s6, s10\n"
    "s_mov_b32 s7, s11\n"
    "buffer_store_dwordx4 v[6:9], v4, s[4:7], 0 offen sc1\n"
    "s_mul_i32 s8, s17, 12\n"
    "v_min_u32_e32 v4, 11, v2\n"
    "v_add_lshl_u32 v4, s8, v4, 7\n"
    "s_mov_b32 s8, s94\n"
    "s_addk_i32 s8, 0xfbfc\n"
    "v_min_i32_e32 v4, s8, v4\n"
    "s_add_i32 s18, s18, 0x1b400\n"
    "global_load_dword v4, v4, s[2:3] offset:1024\n"
    "v_lshl_or_b32 v5, v2, 3, s18\n"
    "s_lshl_b32 s2, s13, 11\n"
    "v_add_u32_e32 v5, v5, v3\n"
    "v_mov_b32_e32 v6, 0x7ffffff0\n"
    "v_cmp_gt_u32_e32 vcc, 4, v2\n"
    "s_add_i32 s2, s2, 0x13000\n"
    "v_add_u32_e32 v3, s2, v3\n"
    "v_cndmask_b32_e32 v7, v6, v5, vcc\n"
    "v_or_b32_e32 v5, s17, v2\n"
    "v_cmp_eq_u32_e32 vcc, 0, v2\n"
    "s_lshl_b32 s3, s13, 7\n"
    "s_or_b32 s3, s3, 0x17000\n"
    "v_cndmask_b32_e32 v3, v6, v3, vcc\n"
    "v_cmp_eq_u32_e32 vcc, 0, v5\n"
    "v_lshlrev_b32_e32 v5, 7, v0\n"
    "v_mov_b32_e32 v8, s3\n"
    "v_and_b32_e32 v5, 0x780, v5\n"
    "s_lshl_b32 s8, s16, 1\n"
    "v_cndmask_b32_e32 v6, v6, v8, vcc\n"
    "v_or_b32_e32 v8, s2, v5\n"
    "v_lshlrev_b32_e32 v5, 6, v0\n"
    "v_and_or_b32 v9, v0, 1, s8\n"
    "v_and_b32_e32 v5, 0x780, v5\n"
    "v_lshlrev_b32_e32 v9, 3, v9\n"
    "v_add3_u32 v9, s18, v5, v9\n"
    "v_mov_b32_e32 v5, 0x580\n"
    "v_mov_b32_e32 v10, 0x600\n"
    "v_and_or_b32 v5, v1, 48, v5\n"
    "v_lshl_or_b32 v10, v2, 2, v10\n"
    "s_mov_b32 s9, 0x7f800000\n"
    "s_waitcnt lgkmcnt(0)\n"
    "s_waitcnt vmcnt(0)\n"
    "v_lshrrev_b32_e32 v2, 3, v0\n"
    "v_lshlrev_b32_e32 v0, 9, v0\n"
    "s_load_dwordx2 s[0:1], s[0:1], 0x20\n"
    "v_and_b32_e32 v0, 0x6000, v0\n"
    "v_and_or_b32 v2, v2, 1, s8\n"
    "v_lshl_or_b32 v0, s13, 19, v0\n"
    "s_lshl_b32 s8, s14, 7\n"
    "v_and_b32_e32 v1, 0x70, v1\n"
    "v_or3_b32 v0, v0, s8, v1\n"
    "s_lshl_b32 s8, s12, 12\n"
    "s_and_b32 s8, s8, 0x1000\n"
    "s_or_b32 s11, s8, 0x4000\n"
    "s_waitcnt lgkmcnt(0)\n"
    "s_and_b32 s1, s1, 0xffff\n"
    "s_mov_b32 s3, 0x20000\n"
    "s_mov_b32 s2, 0x400000\n"
    "v_lshl_add_u32 v0, v2, 17, v0\n"
    "s_waitcnt lgkmcnt(0)\n"
    "s_barrier\n"
    "s_sleep 1\n"
    "s_mov_b32 m0, s11\n"
    "s_nop 0\n"
    "buffer_load_dwordx4 v0, s[0:3], s8 offen nt lds\n"
    "buffer_load_dwordx4 v0, s[0:3], s8 offen offset:1024 nt lds\n"
    "buffer_load_dwordx4 v0, s[0:3], s8 offen offset:2048 nt lds\n"
    "buffer_load_dwordx4 v0, s[0:3], s8 offen offset:3072 nt lds\n"
    "s_xor_b32 s11, s8, 0x1000\n"
    "s_or_b32 s12, s11, 0x4000\n"
    "s_mov_b32 m0, s12\n"
    "s_nop 0\n"
    "buffer_load_dwordx4 v0, s[0:3], s11 offen nt lds\n"
    "buffer_load_dwordx4 v0, s[0:3], s11 offen offset:1024 nt lds\n"
    "buffer_load_dwordx4 v0, s[0:3], s11 offen offset:2048 nt lds\n"
    "buffer_load_dwordx4 v0, s[0:3], s11 offen offset:3072 nt lds\n"
    "s_add_i32 s10, s15, 1\n"
    "s_or_b32 s12, s8, 0x6000\n"
    "s_or_b32 s13, s8, 0x8000\n"
    "s_mov_b32 m0, s12\n"
    "s_nop 0\n"
    "buffer_load_dwordx4 v0, s[0:3], s13 offen nt lds\n"
    "buffer_load_dwordx4 v0, s[0:3], s13 offen offset:1024 nt lds\n"
    "buffer_load_dwordx4 v0, s[0:3], s13 offen offset:2048 nt lds\n"
    "buffer_load_dwordx4 v0, s[0:3], s13 offen offset:3072 nt lds\n"
    "s_xor_b32 s12, s8, 0x7000\n"
    "s_or_b32 s14, s11, 0x8000\n"
    "s_mov_b32 m0, s12\n"
    "s_nop 0\n"
    "buffer_load_dwordx4 v0, s[0:3], s14 offen nt lds\n"
    "buffer_load_dwordx4 v0, s[0:3], s14 offen offset:1024 nt lds\n"
    "buffer_load_dwordx4 v0, s[0:3], s14 offen offset:2048 nt lds\n"
    "buffer_load_dwordx4 v0, s[0:3], s14 offen offset:3072 nt lds\n"
    "v_mov_b32_e32 v11, s10\n"
    "s_waitcnt lgkmcnt(0)\n"
    "s_barrier\n"
    "buffer_store_dword v11, v3, s[4:7], 0 offen sc1\n"
    "s_or_b32 s14, s8, 0x10000\n"
    "s_mov_b32 m0, s13\n"
    "s_nop 0\n"
    "buffer_load_dwordx4 v0, s[0:3], s14 offen nt lds\n"
    "buffer_load_dwordx4 v0, s[0:3], s14 offen offset:1024 nt lds\n"
    "buffer_load_dwordx4 v0, s[0:3], s14 offen offset:2048 nt lds\n"
    "buffer_load_dwordx4 v0, s[0:3], s14 offen offset:3072 nt lds\n"
    "s_xor_b32 s13, s8, 0x5000\n"
    "s_addk_i32 s13, 0x4000\n"
    "s_or_b32 s14, s11, 0x10000\n"
    "s_mov_b32 m0, s13\n"
    "s_nop 0\n"
    "buffer_load_dwordx4 v0, s[0:3], s14 offen nt lds\n"
    "buffer_load_dwordx4 v0, s[0:3], s14 offen offset:1024 nt lds\n"
    "buffer_load_dwordx4 v0, s[0:3], s14 offen offset:2048 nt lds\n"
    "buffer_load_dwordx4 v0, s[0:3], s14 offen offset:3072 nt lds\n"
    "s_or_b32 s13, s8, 0xa000\n"
    "s_or_b32 s8, s8, 0x18000\n"
    "s_mov_b32 m0, s13\n"
    "s_nop 0\n"
    "buffer_load_dwordx4 v0, s[0:3], s8 offen nt lds\n"
    "buffer_load_dwordx4 v0, s[0:3], s8 offen offset:1024 nt lds\n"
    "buffer_load_dwordx4 v0, s[0:3], s8 offen offset:2048 nt lds\n"
    "buffer_load_dwordx4 v0, s[0:3], s8 offen offset:3072 nt lds\n"
    "s_waitcnt lgkmcnt(0)\n"
    "s_barrier\n"
    "s_addk_i32 s12, 0x4000\n"
    "s_or_b32 s8, s11, 0x18000\n"
    "s_mov_b32 m0, s12\n"
    "s_nop 0\n"
    "buffer_load_dwordx4 v0, s[0:3], s8 offen nt lds\n"
    "buffer_load_dwordx4 v0, s[0:3], s8 offen offset:1024 nt lds\n"
    "buffer_load_dwordx4 v0, s[0:3], s8 offen offset:2048 nt lds\n"
    "buffer_load_dwordx4 v0, s[0:3], s8 offen offset:3072 nt lds\n"
    "ds_read_b128 v[0:3], v5\n"
    "s_waitcnt lgkmcnt(0)\n"
    "v_med3_f32 v0, v0, v1, s9\n"
    "v_med3_f32 v1, v2, v3, s9\n"
    "v_med3_f32 v0, v0, v1, s9\n"
    "v_cvt_pk_bf16_f32 v0, 0, v0\n"
    "v_mul_f32_e32 v0, 0x3db504f3, v0\n"
    "v_cvt_pk_bf16_f32 v0, 0, v0\n"
    "v_mul_f32_e32 v0, 0x3fb8aa3b, v0\n"
    "v_min_u32_e32 v4, -2, v0\n"
    "s_waitcnt lgkmcnt(0)\n"
    "s_barrier\n"
    "ds_read_b128 v[0:3], v5 offset:64\n"
    "s_waitcnt lgkmcnt(0)\n"
    "v_add_f32_e32 v0, v0, v1\n"
    "v_add_f32_e32 v1, v2, v3\n"
    "v_add_f32_e32 v0, v0, v1\n"
    "v_min_u32_e32 v5, -2, v0\n"
    "buffer_store_dwordx2 v[4:5], v7, s[4:7], 0 offen sc1\n"
    "s_mov_b32 s0, 0x20000\n"
    "1:\n"
    "buffer_load_dword v0, v8, s[4:7], 0 offen sc1\n"
    "s_waitcnt vmcnt(0)\n"
    "v_cmp_ne_u32 vcc, s10, v0\n"
    "s_cbranch_vccz 2f\n"
    "s_mov_b64 exec, vcc\n"
    "s_sub_u32 s0, s0, 1\n"
    "s_cbranch_scc0 1b\n"
    "2:\n"
    "s_mov_b64 exec, -1\n"
    "buffer_store_dword v11, v6, s[4:7], 0 offen sc1\n"
    "s_sleep 3\n"
    "s_waitcnt lgkmcnt(0)\n"
    "s_barrier\n"
    "s_mov_b32 s0, 0x20000\n"
    "1:\n"
    "buffer_load_dwordx2 v[224:225], v9, s[4:7], 0 offen offset:0 sc1\n"
    "s_waitcnt vmcnt(0)\n"
    "v_max_u32 v0, v224, v225\n"
    "v_cmp_eq_u32 vcc, -1, v0\n"
    "s_cbranch_vccz 2f\n"
    "s_mov_b64 exec, vcc\n"
    "s_sub_u32 s0, s0, 1\n"
    "s_cbranch_scc0 1b\n"
    "2:\n"
    "s_mov_b64 exec, -1\n"
    "s_nop 1\n"
    "v_mov_b32_dpp v0, v224 quad_perm:[2,3,0,1] row_mask:0xf bank_mask:0xf bound_ctrl:1\n"
    "v_med3_f32 v0, v224, v0, s9\n"
    "s_nop 1\n"
    "v_mov_b32_dpp v1, v0 row_ror:4 row_mask:0xf bank_mask:0xf bound_ctrl:1\n"
    "v_med3_f32 v0, v0, v1, s9\n"
    "s_nop 1\n"
    "v_mov_b32_dpp v1, v0 row_ror:8 row_mask:0xf bank_mask:0xf bound_ctrl:1\n"
    "v_med3_f32 v0, v0, v1, s9\n"
    "v_mov_b32_e32 v1, v0\n"
    "s_nop 1\n"
    "v_permlane16_swap_b32_e32 v0, v1\n"
    "v_med3_f32 v0, v0, v1, s9\n"
    "v_sub_f32_e32 v0, v224, v0\n"
    "v_exp_f32_e32 v0, v0\n"
    "s_nop 0\n"
    "v_mul_f32_e32 v1, v225, v0\n"
    "s_nop 1\n"
    "v_mov_b32_dpp v1, v1 quad_perm:[2,3,0,1] row_mask:0xf bank_mask:0xf bound_ctrl:1\n"
    "v_fmac_f32_e32 v1, v225, v0\n"
    "s_nop 1\n"
    "v_add_f32_dpp v1, v1, v1 row_ror:4 row_mask:0xf bank_mask:0xf bound_ctrl:1\n"
    "s_nop 1\n"
    "v_add_f32_dpp v1, v1, v1 row_ror:8 row_mask:0xf bank_mask:0xf bound_ctrl:1\n"
    "v_mov_b32_e32 v2, v1\n"
    "s_nop 1\n"
    "v_permlane16_swap_b32_e32 v1, v2\n"
    "v_add_f32_e32 v1, v1, v2\n"
    "v_rcp_f32_e32 v1, v1\n"
    "s_nop 0\n"
    "v_mul_f32_e32 v0, v0, v1\n"
    "ds_write_b32 v10, v0\n"
    "s_waitcnt lgkmcnt(0)\n"
    "s_barrier\n"
    "s_endpgm\n"
    ".p2align 6\n"
    ".Lfb_role_qh:\n"
    "s_load_dwordx2 s[4:5], s[0:1], 0x58\n"
    "s_ashr_i32 s6, s2, 3\n"
    "s_and_b32 s3, s2, 7\n"
    "s_lshl_b32 s12, s3, 10\n"
    "v_and_b32_e32 v8, 63, v0\n"
    "s_waitcnt lgkmcnt(0)\n"
    "s_mov_b32 s8, s4\n"
    "s_load_dword s7, s[4:5], 0x0\n"
    "s_lshl_b32 s4, s6, 8\n"
    "s_and_b32 s4, s4, 0x300\n"
    "s_or_b32 s13, s4, s12\n"
    "s_lshl_b32 s4, s6, 11\n"
    "s_and_b32 s14, s4, 0xffffe000\n"
    "s_or_b32 s4, s13, s14\n"
    "v_lshl_add_u32 v1, v8, 4, s4\n"
    "v_add_u32_e32 v2, 0x7f400, v1\n"
    "v_mov_b32_e32 v1, 0x7ffffff0\n"
    "v_cmp_gt_u32_e32 vcc, 16, v8\n"
    "s_mov_b32 s11, 0x20000\n"
    "s_mov_b32 s10, 0x83400\n"
    "v_cndmask_b32_e32 v6, v1, v2, vcc\n"
    "v_mov_b32_e32 v2, -1\n"
    "s_and_b32 s9, s5, 0xffff\n"
    "v_mov_b32_e32 v3, v2\n"
    "v_mov_b32_e32 v4, v2\n"
    "v_mov_b32_e32 v5, v2\n"
    "buffer_store_dwordx4 v[2:5], v6, s[8:11], 0 offen sc1\n"
    "s_lshl_b32 s3, s3, 6\n"
    "s_and_b32 s4, s2, -8\n"
    "v_lshlrev_b32_e32 v6, 2, v8\n"
    "s_add_i32 s15, s3, s4\n"
    "v_and_b32_e32 v10, 4, v6\n"
    "v_or_b32_e32 v11, s15, v10\n"
    "v_lshrrev_b32_e32 v2, 4, v0\n"
    "v_and_or_b32 v12, v2, 2, v11\n"
    "s_load_dwordx2 s[4:5], s[0:1], 0x0\n"
    "v_and_or_b32 v2, v2, 1, v12\n"
    "v_lshlrev_b32_e32 v13, 1, v2\n"
    "s_waitcnt lgkmcnt(0)\n"
    "global_load_ushort v14, v13, s[4:5]\n"
    "s_lshl_b32 s4, s6, 7\n"
    "s_add_i32 s4, s12, s4\n"
    "s_add_i32 s4, s4, 0x11000\n"
    "v_mov_b32_e32 v2, s4\n"
    "v_cmp_eq_u32_e64 s[4:5], 0, v8\n"
    "s_add_i32 s3, s7, 1\n"
    "v_mov_b32_e32 v3, s3\n"
    "v_cndmask_b32_e64 v2, v1, v2, s[4:5]\n"
    "s_waitcnt lgkmcnt(0)\n"
    "s_barrier\n"
    "s_waitcnt vmcnt(0)\n"
    "buffer_store_dword v3, v2, s[8:11], 0 offen sc1\n"
    "s_waitcnt lgkmcnt(0)\n"
    "s_barrier\n"
    "s_waitcnt lgkmcnt(0)\n"
    "s_barrier\n"
    "ds_read2st64_b32 v[2:3], v6 offset0:8 offset1:9\n"
    "ds_read2st64_b32 v[4:5], v6 offset0:10 offset1:11\n"
    "v_or_b32_e32 v6, s14, v6\n"
    "v_or_b32_e32 v6, s13, v6\n"
    "v_add_u32_e32 v15, 0x7f000, v6\n"
    "s_waitcnt lgkmcnt(1)\n"
    "v_mov_b32_e32 v6, v2\n"
    "s_waitcnt lgkmcnt(0)\n"
    "v_mov_b32_e32 v7, v4\n"
    "v_mov_b32_e32 v4, v3\n"
    "v_pk_add_f32 v[2:3], v[6:7], v[4:5]\n"
    "v_bfe_u32 v9, v0, 1, 5\n"
    "v_pk_add_f32 v[2:3], v[2:3], v[2:3] op_sel:[0,1] op_sel_hi:[1,0]\n"
    "s_movk_i32 s14, 0x400\n"
    "v_min_u32_e32 v2, -2, v2\n"
    "buffer_store_dword v2, v15, s[8:11], 0 offen offset:1024 sc1\n"
    "v_lshlrev_b32_e32 v2, 7, v8\n"
    "v_add_u32_e32 v3, 0x11000, v2\n"
    "s_mov_b32 s6, 0x20000\n"
    "1:\n"
    "buffer_load_dword v4, v3, s[8:11], 0 offen sc1\n"
    "s_waitcnt vmcnt(0)\n"
    "v_cmp_ne_u32 vcc, s3, v4\n"
    "s_cbranch_vccz 2f\n"
    "s_mov_b64 exec, vcc\n"
    "s_sub_u32 s6, s6, 1\n"
    "s_cbranch_scc0 1b\n"
    "2:\n"
    "s_mov_b64 exec, -1\n"
    "v_lshlrev_b32_e32 v3, 2, v12\n"
    "v_add_u32_e32 v4, 0x7f400, v3\n"
    "v_add_u32_e32 v3, 0x81400, v3\n"
    "s_movk_i32 s6, 0x1800\n"
    "s_mov_b32 s7, 0x20000\n"
    "1:\n"
    "buffer_load_dwordx2 v[224:225], v4, s[8:11], 0 offen offset:0 sc1\n"
    "buffer_load_dwordx2 v[226:227], v3, s[8:11], 0 offen offset:0 sc1\n"
    "buffer_load_dwordx2 v[228:229], v4, s[8:11], 0 offen offset:2048 sc1\n"
    "buffer_load_dwordx2 v[230:231], v3, s[8:11], 0 offen offset:2048 sc1\n"
    "buffer_load_dwordx2 v[232:233], v4, s[8:11], s6 offen offset:0 sc1\n"
    "buffer_load_dwordx2 v[234:235], v3, s[8:11], s6 offen offset:0 sc1\n"
    "s_waitcnt vmcnt(0)\n"
    "v_max3_u32 v5, v224, v225, v226\n"
    "v_max3_u32 v5, v5, v227, v228\n"
    "v_max3_u32 v5, v5, v229, v230\n"
    "v_max3_u32 v5, v5, v231, v232\n"
    "v_max3_u32 v5, v5, v233, v234\n"
    "v_max3_u32 v5, v5, v235, v235\n"
    "v_cmp_eq_u32 vcc, -1, v5\n"
    "s_cbranch_vccz 2f\n"
    "s_mov_b64 exec, vcc\n"
    "s_sub_u32 s7, s7, 1\n"
    "s_cbranch_scc0 1b\n"
    "2:\n"
    "s_mov_b64 exec, -1\n"
    "v_and_b32_e32 v3, 16, v0\n"
    "v_cmp_eq_u32_e64 s[6:7], 0, v3\n"
    "v_and_b32_e32 v2, 0x380, v2\n"
    "v_or_b32_e32 v2, 0x17000, v2\n"
    "v_cndmask_b32_e64 v3, v225, v224, s[6:7]\n"
    "v_cndmask_b32_e64 v5, v227, v226, s[6:7]\n"
    "v_add_f32_e32 v3, v3, v5\n"
    "v_cvt_pk_bf16_f32 v6, 0, v3\n"
    "v_cndmask_b32_e64 v3, v233, v232, s[6:7]\n"
    "v_cndmask_b32_e64 v5, v235, v234, s[6:7]\n"
    "v_add_f32_e32 v3, v3, v5\n"
    "v_cvt_pk_bf16_f32 v7, 0, v3\n"
    "s_mov_b32 s12, 0x20000\n"
    "1:\n"
    "buffer_load_dword v3, v2, s[8:11], 0 offen sc1\n"
    "s_waitcnt vmcnt(0)\n"
    "v_cmp_ne_u32 vcc, s3, v3\n"
    "s_cbranch_vccz 2f\n"
    "s_mov_b64 exec, vcc\n"
    "s_sub_u32 s12, s12, 1\n"
    "s_cbranch_scc0 1b\n"
    "2:\n"
    "s_mov_b64 exec, -1\n"
    "v_bfe_u32 v2, v8, 1, 3\n"
    "v_lshlrev_b32_e32 v3, 2, v11\n"
    "s_mov_b32 s12, 0x9800\n"
    "v_mad_u32_u24 v2, v2, s12, v3\n"
    "v_and_b32_e32 v4, 32, v0\n"
    "s_sleep 14\n"
    "v_add_u32_e32 v3, 0x1bc00, v2\n"
    "v_add_u32_e32 v2, 0x1c400, v2\n"
    "s_mov_b32 s12, 0x20000\n"
    "1:\n"
    "buffer_load_dwordx4 v[224:227], v3, s[8:11], 0 offen sc1\n"
    "buffer_load_dwordx4 v[228:231], v2, s[8:11], 0 offen sc1\n"
    "s_waitcnt vmcnt(0)\n"
    "v_max3_u32 v5, v224, v225, v226\n"
    "v_max3_u32 v5, v5, v227, v228\n"
    "v_max3_u32 v5, v5, v229, v230\n"
    "v_max3_u32 v5, v5, v231, v231\n"
    "v_cmp_eq_u32 vcc, -1, v5\n"
    "s_cbranch_vccz 2f\n"
    "s_mov_b64 exec, vcc\n"
    "s_sub_u32 s12, s12, 1\n"
    "s_cbranch_scc0 1b\n"
    "2:\n"
    "s_mov_b64 exec, -1\n"
    "v_and_b32_e32 v0, 14, v0\n"
    "v_cndmask_b32_e64 v2, v227, v226, s[6:7]\n"
    "v_cndmask_b32_e64 v3, v231, v230, s[6:7]\n"
    "v_cndmask_b32_e64 v5, v225, v224, s[6:7]\n"
    "v_cndmask_b32_e64 v11, v229, v228, s[6:7]\n"
    "v_cmp_eq_u32_e32 vcc, 0, v4\n"
    "s_load_dwordx2 s[12:13], s[0:1], 0x50\n"
    "s_waitcnt lgkmcnt(0)\n"
    "s_and_b32 s13, s13, 0xffff\n"
    "v_cndmask_b32_e32 v3, v3, v11, vcc\n"
    "v_cndmask_b32_e32 v2, v2, v5, vcc\n"
    "v_cmp_gt_u32_e32 vcc, 48, v8\n"
    "v_mov_b32_dpp v5, v3 quad_perm:[2,3,0,1] row_mask:0xf bank_mask:0xf bound_ctrl:1\n"
    "v_mov_b32_dpp v4, v2 quad_perm:[2,3,0,1] row_mask:0xf bank_mask:0xf bound_ctrl:1\n"
    "v_pk_add_f32 v[2:3], v[2:3], v[4:5]\n"
    "s_nop 1\n"
    "v_mov_b32_dpp v4, v2 row_shl:4 row_mask:0xf bank_mask:0xf bound_ctrl:1\n"
    "v_mov_b32_dpp v5, v3 row_shl:4 row_mask:0xf bank_mask:0xf bound_ctrl:1\n"
    "v_pk_add_f32 v[2:3], v[2:3], v[4:5]\n"
    "s_nop 1\n"
    "v_mov_b32_dpp v4, v2 row_ror:8 row_mask:0xf bank_mask:0xf bound_ctrl:1\n"
    "v_mov_b32_dpp v5, v3 row_ror:8 row_mask:0xf bank_mask:0xf bound_ctrl:1\n"
    "v_pk_add_f32 v[2:3], v[2:3], v[4:5]\n"
    "v_lshlrev_b32_e32 v4, 16, v14\n"
    "v_pk_add_f32 v[2:3], v[2:3], v[2:3] op_sel_hi:[0,1]\n"
    "v_cvt_pk_bf16_f32 v2, 0, v3\n"
    "v_mul_f32_e32 v2, v6, v2\n"
    "v_cvt_pk_bf16_f32 v2, 0, v2\n"
    "v_add_f32_e32 v2, v4, v2\n"
    "v_cvt_pk_bf16_f32 v6, 0, v2\n"
    "v_lshl_add_u32 v2, v9, 9, s15\n"
    "v_or_b32_e32 v2, v2, v10\n"
    "v_lshlrev_b32_e32 v2, 2, v2\n"
    "v_add_u32_e32 v3, 0x63400, v2\n"
    "v_add_u32_e32 v2, 0x73400, v2\n"
    "v_cndmask_b32_e32 v2, v1, v2, vcc\n"
    "v_cmp_eq_u32_e32 vcc, 0, v0\n"
    "s_mov_b32 s15, s11\n"
    "s_nop 0\n"
    "v_cndmask_b32_e32 v0, v1, v13, vcc\n"
    "s_sleep 24\n"
    "s_mov_b32 s0, 0x20000\n"
    "1:\n"
    "buffer_load_dwordx4 v[224:227], v3, s[8:11], 0 offen sc1\n"
    "buffer_load_dwordx4 v[228:231], v2, s[8:11], 0 offen sc1\n"
    "s_waitcnt vmcnt(0)\n"
    "v_max3_u32 v4, v224, v225, v226\n"
    "v_max3_u32 v4, v4, v227, v228\n"
    "v_max3_u32 v4, v4, v229, v230\n"
    "v_max3_u32 v4, v4, v231, v231\n"
    "v_cmp_eq_u32 vcc, -1, v4\n"
    "s_cbranch_vccz 2f\n"
    "s_mov_b64 exec, vcc\n"
    "s_sub_u32 s0, s0, 1\n"
    "s_cbranch_scc0 1b\n"
    "2:\n"
    "s_mov_b64 exec, -1\n"
    "s_cmp_lg_u32 s2, 0\n"
    "v_pk_add_f32 v[2:3], v[226:227], v[230:231]\n"
    "v_pk_add_f32 v[4:5], v[224:225], v[228:229]\n"
    "s_nop 1\n"
    "v_permlane32_swap_b32_e32 v4, v2\n"
    "v_permlane32_swap_b32_e32 v5, v3\n"
    "v_add_f32_e32 v2, v4, v2\n"
    "v_add_f32_e32 v3, v5, v3\n"
    "s_nop 1\n"
    "v_permlane16_swap_b32_e32 v2, v3\n"
    "v_add_f32_e32 v2, v2, v3\n"
    "s_nop 1\n"
    "v_add_f32_dpp v2, v2, v2 row_ror:8 row_mask:0xf bank_mask:0xf bound_ctrl:1\n"
    "s_nop 1\n"
    "v_add_f32_dpp v2, v2, v2 row_ror:4 row_mask:0xf bank_mask:0xf bound_ctrl:1\n"
    "s_nop 1\n"
    "v_add_f32_dpp v2, v2, v2 quad_perm:[2,3,0,1] row_mask:0xf bank_mask:0xf bound_ctrl:1\n"
    "v_cvt_pk_bf16_f32 v2, 0, v2\n"
    "v_mul_f32_e32 v2, v7, v2\n"
    "v_cvt_pk_bf16_f32 v2, 0, v2\n"
    "v_add_f32_e32 v2, v6, v2\n"
    "v_cvt_pk_bf16_f32 v2, v2, s0\n"
    "buffer_store_short v2, v0, s[12:15], 0 offen sc1\n"
    "s_cbranch_scc1 .Lfb_role_qh_2\n"
    "v_cndmask_b32_e64 v0, v1, 0, s[4:5]\n"
    "v_mov_b32_e32 v1, s3\n"
    "buffer_store_dword v1, v0, s[8:11], 0 offen sc1\n"
    ".Lfb_role_qh_2:\n"
    "s_endpgm\n"
    ".Lfb_end:\n"
    : "+{s[0:1]}"(ka), "+{s2}"(bid), "+{v0}"(tid) : "i"((__attribute__((address_space(3))) unsigned char*)lds) : "memory", "vcc", "scc", "s95", "v255");
#endif
}

extern "C" void kernel_launch(void* const* d_in, const int* in_sizes, int n_in, void* d_out, int out_size, void* d_ws,
                              size_t ws_size, hipStream_t stream) {
  fused_block<<<256, 320, 0, stream>>>((const u16*)d_in[0], (const u16*)d_in[1], (const u16*)d_in[2], (const u16*)d_in[3],
                                       (const u16*)d_in[4], (const u16*)d_in[6], (const u16*)d_in[7], (const u16*)d_in[8],
                                       (const u16*)d_in[9], (const u16*)d_in[10], (u16*)d_out, (unsigned char*)d_ws);
}
```
